```python
import torch

def _softmax(x, axis=-1):
    return torch.softmax(x, dim=axis)
def _gelu(x):
    return torch.nn.functional.gelu(x, approximate="tanh")
def _silu(x):
    return torch.nn.functional.silu(x)
def _sigmoid(x):
    return torch.sigmoid(x)
def _layernorm(x, axis=-1, eps=1e-5):
    m = x.mean(dim=axis, keepdim=True)
    v = x.var(dim=axis, keepdim=True, unbiased=False)
    return (x - m) / torch.sqrt(v + eps)
def _scatter_add(tgt, idx, src, axis=0):
    return tgt.clone().index_add_(axis, idx, src)
def _segment_sum(src, seg, n):
    out = torch.zeros((n,) + src.shape[1:], dtype=src.dtype)
    return out.index_add_(0, seg, src)
def _pad(x, pw):
    flat = tuple(v for pair in reversed(pw) for v in pair)
    return torch.nn.functional.pad(x, flat)

kernel_name = 'sequential_preLN__gqa_block_cached__glu_gated_bilinear__decode'

def setup_inputs():

    _w = (lambda a: (lambda t: t.to(torch.bfloat16) if t.is_floating_point() else t)(torch.as_tensor(a)))
    def _A_setup():
        B, Sq, D = 8, 1, 448
        Hq, Hkv, Sk, Dh = 16, 8, 8192, 256
        assert Hq % Hkv == 0, (Hq, Hkv)
        return {
            "x": _w((lambda *s: torch.randn(*s, dtype=torch.bfloat16) * 0.05)(B, Sq, D)),
            "k_cache": _w((lambda *s: torch.randn(*s, dtype=torch.bfloat16) * 1.6)(B, Hkv, Sk, Dh)),
            "v_cache": _w((lambda *s: torch.randn(*s, dtype=torch.bfloat16) * 0.8)(B, Hkv, Sk, Dh)),
            "wq": _w((lambda *s: torch.randn(*s, dtype=torch.bfloat16) * 1.6)(D, Hq * Dh) * (D ** -0.5)),
            "wo": _w((lambda *s: torch.randn(*s, dtype=torch.bfloat16) * 0.8)(Hq * Dh, D) * ((Hq * Dh) ** -0.5)),
        }
    _A_in = _A_setup()

    def _B_setup():
        B, S, D, F = 8, 1, 448, 1792
        return {
            "x": _w((lambda *s: torch.randn(*s, dtype=torch.bfloat16) * 0.05)(B, S, D)),
            "w_gate": _w((lambda *s: torch.randn(*s, dtype=torch.bfloat16) * 0.4)(D, F) * (D ** -0.5)),
            "w_up": _w((lambda *s: torch.randn(*s, dtype=torch.bfloat16) * 0.4)(D, F) * (D ** -0.5)),
            "w_down": _w((lambda *s: torch.randn(*s, dtype=torch.bfloat16) * 0.4)(F, D) * (F ** -0.5)),
        }
    _B_in = _B_setup()

    return {
        "A_x": _A_in["x"],
        "A_k_cache": _A_in["k_cache"],
        "A_v_cache": _A_in["v_cache"],
        "A_wq": _A_in["wq"],
        "A_wo": _A_in["wo"],
        "B_x": _B_in["x"],
        "B_w_gate": _B_in["w_gate"],
        "B_w_up": _B_in["w_up"],
        "B_w_down": _B_in["w_down"],
    }

def reference(A_x, A_k_cache, A_v_cache, A_wq, A_wo, B_x, B_w_gate, B_w_up, B_w_down):
    def _A_ref(x, k_cache, v_cache, wq, wo):
        B, Sq, D = x.shape
        Hq, Dh = 16, 256
        Hkv = k_cache.shape[1]
        rep = Hq // Hkv
        q = (lambda x, *d: x.permute(*d))((x @ wq).reshape(B, Sq, Hq, Dh), 0, 2, 1, 3)
        k = (lambda x, n, axis: torch.repeat_interleave(x, n, dim=axis))(k_cache, rep, axis=1)
        v = (lambda x, n, axis: torch.repeat_interleave(x, n, dim=axis))(v_cache, rep, axis=1)
        scores = torch.einsum("bhqd,bhkd->bhqk", q, k) * (Dh ** -0.5)
        p = _softmax(scores, axis=-1)
        o = torch.einsum("bhqk,bhkd->bhqd", p, v)
        return (lambda x, *d: x.permute(*d))(o, 0, 2, 1, 3).reshape(B, Sq, Hq * Dh) @ wo

    def _B_ref(x, w_gate, w_up, w_down):
        g = torch.einsum("bsd,df->bsf", x, w_gate)
        u = torch.einsum("bsd,df->bsf", x, w_up)
        h = g * u
        return torch.einsum("bsf,fd->bsd", h, w_down)

    x = A_x
    h = x + _A_ref(_layernorm(x, axis=-1), A_k_cache, A_v_cache, A_wq, A_wo)
    h2 = h + _B_ref(_layernorm(h, axis=-1), B_w_gate, B_w_up, B_w_down)
    return h2
```

```cpp
#include <hip/hip_runtime.h>
#include <hip/hip_bf16.h>
#include <stdint.h>

typedef unsigned v4u __attribute__((ext_vector_type(4)));
typedef float f32x4 __attribute__((ext_vector_type(4)));
typedef short bf16x8 __attribute__((ext_vector_type(8)));
typedef unsigned short u16;

constexpr int NB = 8, DM = 448, HQ = 16, HKV = 8, SK = 8192, DH = 256, FF = 1792;
constexpr int NQ = HQ * DH;

__device__ __forceinline__ float bf2f(u16 b) { return __builtin_bit_cast(float, (unsigned)b << 16); }
typedef __bf16 bf16x2_t __attribute__((ext_vector_type(2)));
typedef float f32x2 __attribute__((ext_vector_type(2)));
__device__ __forceinline__ unsigned cvtpk(float lo, float hi) {
  return __builtin_bit_cast(unsigned, __builtin_convertvector(f32x2{lo, hi}, bf16x2_t));
}
__device__ __forceinline__ float rbf(float x) { return __builtin_bit_cast(float, cvtpk(x, x) << 16); }
__device__ __forceinline__ u16 f2bf(float x) { return (u16)(cvtpk(x, x) & 0xffffu); }
__device__ __forceinline__ f32x4 mfma(v4u a, v4u b, f32x4 c) {
  return __builtin_amdgcn_mfma_f32_16x16x32_bf16(__builtin_bit_cast(bf16x8, a), __builtin_bit_cast(bf16x8, b), c, 0, 0, 0);
}
template <int CTRL> __device__ __forceinline__ float dpp(float x) {
  return __builtin_bit_cast(float, __builtin_amdgcn_mov_dpp(__builtin_bit_cast(int, x), CTRL, 0xf, 0xf, true));
}
template <int CTRL> __device__ __forceinline__ unsigned dppu(unsigned x) {
  return (unsigned)__builtin_amdgcn_mov_dpp((int)x, CTRL, 0xf, 0xf, true);
}
constexpr int XOR1 = 0xB1, XOR2 = 0x4E, HMIR = 0x141;
__device__ __forceinline__ float xrow_max(float x) {
  auto s = __builtin_amdgcn_permlane16_swap(__float_as_uint(x), __float_as_uint(x), false, false);
  x = fmaxf(__uint_as_float(s[0]), __uint_as_float(s[1]));
  auto t = __builtin_amdgcn_permlane32_swap(__float_as_uint(x), __float_as_uint(x), false, false);
  return fmaxf(__uint_as_float(t[0]), __uint_as_float(t[1]));
}
__device__ __forceinline__ float xrow_sum(float x) {
  auto s = __builtin_amdgcn_permlane16_swap(__float_as_uint(x), __float_as_uint(x), false, false);
  x = __uint_as_float(s[0]) + __uint_as_float(s[1]);
  auto t = __builtin_amdgcn_permlane32_swap(__float_as_uint(x), __float_as_uint(x), false, false);
  return __uint_as_float(t[0]) + __uint_as_float(t[1]);
}
__device__ __forceinline__ float wave_sum(float x) {
  x += dpp<XOR1>(x); x += dpp<XOR2>(x); x += dpp<HMIR>(x); x += dpp<0x140>(x);
  return xrow_sum(x);
}

__device__ __forceinline__ void ln7(float (&v)[7]) {
  float s = 0.f;
#pragma unroll
  for (int i = 0; i < 7; ++i) s += v[i];
  const float mean = wave_sum(s) * (1.0f / DM);
  float q = 0.f;
#pragma unroll
  for (int i = 0; i < 7; ++i) { const float d = v[i] - mean; q += d * d; }
  const float var = rbf(wave_sum(q) * (1.0f / DM));
  const float mb = rbf(mean);
  const float sd = rbf(sqrtf(rbf(var + 1e-5f)));
#pragma unroll
  for (int i = 0; i < 7; ++i) v[i] = rbf(rbf(v[i] - mb) / sd);
}

__global__ void __launch_bounds__(256) k_qproj(const u16* __restrict__ x, const u16* __restrict__ wq, u16* __restrict__ q) {
  __shared__ float xn[NB][DM];
  __shared__ float red[4][NB][64];
  const int tid = threadIdx.x, lane = tid & 63, w = tid >> 6;
  for (int b = w; b < NB; b += 4) {
    float v[7];
#pragma unroll
    for (int i = 0; i < 7; ++i) v[i] = bf2f(x[b * DM + lane + 64 * i]);
    ln7(v);
#pragma unroll
    for (int i = 0; i < 7; ++i) xn[b][lane + 64 * i] = v[i];
  }
  __syncthreads();
  const int col = blockIdx.x * 64 + lane;
  float acc[NB];
#pragma unroll
  for (int b = 0; b < NB; ++b) acc[b] = 0.f;
  for (int k = w * 112; k < w * 112 + 112; ++k) {
    const float wv = bf2f(wq[(size_t)k * NQ + col]);
#pragma unroll
    for (int b = 0; b < NB; ++b) acc[b] = fmaf(xn[b][k], wv, acc[b]);
  }
#pragma unroll
  for (int b = 0; b < NB; ++b) red[w][b][lane] = acc[b];
  __syncthreads();
  for (int b = w; b < NB; b += 4) q[b * NQ + col] = f2bf(red[0][b][lane] + red[1][b][lane] + red[2][b][lane] + red[3][b][lane]);
}

constexpr int AW = 8, NS = 2;
__device__ __forceinline__ v4u ldnt(const v4u* p) { return __builtin_nontemporal_load(p); }

__global__ void __launch_bounds__(AW * 64, 2) k_attn(const u16* __restrict__ q, const v4u* __restrict__ K, const v4u* __restrict__ V,
                                                   float* __restrict__ pacc, float* __restrict__ pml) {
  __shared__ float mrg[AW][2][DH];
  __shared__ float mls[AW][2][2];
  const int tid = threadIdx.x, lane = tid & 63, w = __builtin_amdgcn_readfirstlane(tid >> 6);
  const int wg = blockIdx.x;
  const int pair = (wg >> 3) * 2 + ((wg >> 2) & 1), sp = wg & 3;
  const int b = pair >> 3, kvh = pair & 7;
  const int g = lane >> 4, r = lane & 15;
  constexpr int NST = SK / 4 / 8 / AW;
  const size_t base = ((size_t)pair * (SK / 8) + (size_t)sp * (SK / 32) + w) * 256;
  const v4u* kp = K + base + ((r >> 2) * 32 + g * 4 + (r & 3));
  const v4u* vp = V + base + (g * 32 + r);
  v4u kb[NS][4], vb[NS][4];
#define ISSUE_K(S, P) do { _Pragma("unroll") for (int i = 0; i < 4; ++i) kb[S][i] = ldnt(kp + (size_t)(P) * (AW * 256) + (i >> 1) * 128 + (i & 1) * 16); } while (0)
#define ISSUE_V(S, P) do { _Pragma("unroll") for (int i = 0; i < 4; ++i) vb[S][i] = ldnt(vp + (size_t)(P) * (AW * 256) + (i >> 1) * 128 + (i & 1) * 16); } while (0)
  const int h = r >> 3, etal = (r >> 2) & 1, sig = r & 3;
  v4u qb[2][2];
  {
    const v4u* qp = (const v4u*)(q + (size_t)b * NQ + (kvh * 2 + h) * DH + 32 * g + 8 * sig);
    const v4u q0 = qp[0], q1 = qp[16];
    const v4u z = {0, 0, 0, 0};
    qb[0][0] = etal == 0 ? q0 : z; qb[0][1] = etal == 1 ? q0 : z;
    qb[1][0] = etal == 0 ? q1 : z; qb[1][1] = etal == 1 ? q1 : z;
  }
#pragma unroll
  for (int s = 0; s < NS; ++s) { ISSUE_K(s, s); ISSUE_V(s, s); }
  __builtin_amdgcn_sched_barrier(0);
  unsigned msk[4];
#pragma unroll
  for (int i = 0; i < 4; ++i) msk[i] = (((lane & 7) >> 1) == i) ? ((lane & 1) ? 0xffff0000u : 0x0000ffffu) : 0u;
  f32x4 o0 = {0, 0, 0, 0}, o1 = {0, 0, 0, 0};
  float m = -INFINITY, l = 0.f;
  constexpr float C2 = 0.0625f * 1.4426950408889634f;
#define STAGE(S, PNEXT, DO_ISSUE) do { \
    f32x4 d = {0, 0, 0, 0}; \
    d = mfma(kb[S][0], qb[0][0], d); d = mfma(kb[S][1], qb[1][0], d); d = mfma(kb[S][2], qb[0][1], d); d = mfma(kb[S][3], qb[1][1], d); \
    if (DO_ISSUE) ISSUE_K(S, PNEXT); \
    float sc = (sig & 2) ? ((sig & 1) ? d[3] : d[2]) : ((sig & 1) ? d[1] : d[0]); \
    sc += dpp<XOR1>(sc); sc += dpp<XOR2>(sc); \
    sc = rbf(sc) * C2; \
    float pm = fmaxf(sc, dpp<HMIR>(sc)); pm = xrow_max(pm); \
    const float mn = fmaxf(m, pm); \
    const float alpha = __builtin_amdgcn_exp2f(m - mn); \
    const float p = __builtin_amdgcn_exp2f(sc - mn); \
    m = mn; l = fmaf(l, alpha, p); \
    o0 *= alpha; o1 *= alpha; \
    const unsigned pw = cvtpk(p, p), pwo = dppu<HMIR>(pw); \
      \
    const v4u b0 = {pw & msk[0], pw & msk[1], pwo & msk[2], pwo & msk[3]}; \
    const v4u b1 = {pwo & msk[0], pwo & msk[1], pw & msk[2], pw & msk[3]}; \
    o0 = mfma(vb[S][0], b0, o0); o1 = mfma(vb[S][1], b0, o1); o0 = mfma(vb[S][2], b1, o0); o1 = mfma(vb[S][3], b1, o1); \
    if (DO_ISSUE) ISSUE_V(S, PNEXT); \
    __builtin_amdgcn_sched_barrier(0); \
  } while (0)
  for (int it = 0; it + NS < NST; it += NS) {
#pragma unroll
    for (int s = 0; s < NS; ++s) STAGE(s, it + NS + s, true);
  }
#pragma unroll
  for (int s = 0; s < NS; ++s) STAGE(s, 0, false);
  l = l + dpp<HMIR>(l); l = xrow_sum(l);
  const int jj = lane & 7;
#pragma unroll
  for (int i = 0; i < 4; ++i) { mrg[w][h][8 * (4 * g + i) + jj] = o0[i]; mrg[w][h][128 + 8 * (4 * g + i) + jj] = o1[i]; }
  if (g == 0 && jj == 0) { mls[w][h][0] = m; mls[w][h][1] = l; }
  __syncthreads();
  {
    const int hh = tid >> 8, d = tid & 255;
    float M = -INFINITY;
#pragma unroll
    for (int ww = 0; ww < AW; ++ww) M = fmaxf(M, mls[ww][hh][0]);
    float L = 0.f, a = 0.f;
#pragma unroll
    for (int ww = 0; ww < AW; ++ww) { const float f = __builtin_amdgcn_exp2f(mls[ww][hh][0] - M); L = fmaf(f, mls[ww][hh][1], L); a = fmaf(f, mrg[ww][hh][d], a); }
    pacc[((size_t)(pair * 4 + sp) * 2 + hh) * DH + d] = a;
    if (d == 0) { pml[((pair * 4 + sp) * 2 + hh) * 2] = M; pml[((pair * 4 + sp) * 2 + hh) * 2 + 1] = L; }
  }
}

__global__ void __launch_bounds__(512) k_wo(const float* __restrict__ pacc, const float* __restrict__ pml, const u16* __restrict__ wo,
                                            const u16* __restrict__ x, u16* __restrict__ hbuf) {
  __shared__ float o[NQ];
  __shared__ float red[8][64];
  const int b = blockIdx.x, tid = threadIdx.x;
  for (int i = tid; i < NQ; i += 512) {
    const int hq = i >> 8, d = i & 255, pair = b * 8 + (hq >> 1), hh = hq & 1;
    float M = -INFINITY;
    for (int s = 0; s < 4; ++s) M = fmaxf(M, pml[((pair * 4 + s) * 2 + hh) * 2]);
    float L = 0.f, a = 0.f;
    for (int s = 0; s < 4; ++s) { const float f = __builtin_amdgcn_exp2f(pml[((pair * 4 + s) * 2 + hh) * 2] - M); L = fmaf(f, pml[((pair * 4 + s) * 2 + hh) * 2 + 1], L); a = fmaf(f, pacc[((size_t)(pair * 4 + s) * 2 + hh) * DH + d], a); }
    o[i] = rbf(a / L);
  }
  __syncthreads();
  const int col = blockIdx.y * 64 + (tid & 63), kq = tid >> 6;
  float acc = 0.f;
  for (int k = kq * 512; k < kq * 512 + 512; ++k) acc = fmaf(o[k], bf2f(wo[(size_t)k * DM + col]), acc);
  red[kq][tid & 63] = acc;
  __syncthreads();
  if (tid < 64) {
    float s = 0.f;
    for (int i = 0; i < 8; ++i) s += red[i][tid];
    hbuf[b * DM + col] = f2bf(bf2f(x[b * DM + col]) + rbf(s));
  }
}

__global__ void __launch_bounds__(256) k_gu(const u16* __restrict__ hbuf, const u16* __restrict__ wg, const u16* __restrict__ wu, u16* __restrict__ hh) {
  __shared__ float xn[NB][DM];
  __shared__ float red[2][4][NB][64];
  const int tid = threadIdx.x, lane = tid & 63, w = tid >> 6;
  for (int b = w; b < NB; b += 4) {
    float v[7];
#pragma unroll
    for (int i = 0; i < 7; ++i) v[i] = bf2f(hbuf[b * DM + lane + 64 * i]);
    ln7(v);
#pragma unroll
    for (int i = 0; i < 7; ++i) xn[b][lane + 64 * i] = v[i];
  }
  __syncthreads();
  const int col = blockIdx.x * 64 + lane;
  float ag[NB], au[NB];
#pragma unroll
  for (int b = 0; b < NB; ++b) { ag[b] = 0.f; au[b] = 0.f; }
  for (int k = w * 112; k < w * 112 + 112; ++k) {
    const float gv = bf2f(wg[(size_t)k * FF + col]), uv = bf2f(wu[(size_t)k * FF + col]);
#pragma unroll
    for (int b = 0; b < NB; ++b) { ag[b] = fmaf(xn[b][k], gv, ag[b]); au[b] = fmaf(xn[b][k], uv, au[b]); }
  }
#pragma unroll
  for (int b = 0; b < NB; ++b) { red[0][w][b][lane] = ag[b]; red[1][w][b][lane] = au[b]; }
  __syncthreads();
  for (int b = w; b < NB; b += 4) {
    const float gg = rbf(red[0][0][b][lane] + red[0][1][b][lane] + red[0][2][b][lane] + red[0][3][b][lane]);
    const float uu = rbf(red[1][0][b][lane] + red[1][1][b][lane] + red[1][2][b][lane] + red[1][3][b][lane]);
    hh[b * FF + col] = f2bf(gg * uu);
  }
}

__global__ void __launch_bounds__(512) k_down(const u16* __restrict__ hh, const u16* __restrict__ wd, const u16* __restrict__ hbuf, u16* __restrict__ out) {
  __shared__ float red[8][64];
  const int b = blockIdx.x, tid = threadIdx.x;
  const int col = blockIdx.y * 64 + (tid & 63), kq = tid >> 6;
  float acc = 0.f;
  for (int k = kq * 224; k < kq * 224 + 224; ++k) acc = fmaf(bf2f(hh[b * FF + k]), bf2f(wd[(size_t)k * DM + col]), acc);
  red[kq][tid & 63] = acc;
  __syncthreads();
  if (tid < 64) {
    float s = 0.f;
    for (int i = 0; i < 8; ++i) s += red[i][tid];
    out[b * DM + col] = f2bf(bf2f(hbuf[b * DM + col]) + rbf(s));
  }
}

extern "C" void kernel_launch(void* const* d_in, const int* in_sizes, int n_in, void* d_out, int out_size, void* d_ws, size_t ws_size,
                              hipStream_t stream) {
  const u16* x = (const u16*)d_in[0];
  const v4u* K = (const v4u*)d_in[1];
  const v4u* V = (const v4u*)d_in[2];
  const u16* wq = (const u16*)d_in[3];
  const u16* wo = (const u16*)d_in[4];
  const u16* wg = (const u16*)d_in[6];
  const u16* wu = (const u16*)d_in[7];
  const u16* wd = (const u16*)d_in[8];
  char* ws = (char*)d_ws;
  u16* q = (u16*)ws;
  float* pacc = (float*)(ws + 65536);
  float* pml = (float*)(ws + 65536 + 524288);
  u16* hbuf = (u16*)(ws + 65536 + 524288 + 4096);
  u16* hh = (u16*)(ws + 65536 + 524288 + 4096 + 8192);
  k_qproj<<<64, 256, 0, stream>>>(x, wq, q);
  k_attn<<<256, AW * 64, 0, stream>>>(q, K, V, pacc, pml);
  k_wo<<<dim3(8, 7), 512, 0, stream>>>(pacc, pml, wo, x, hbuf);
  k_gu<<<28, 256, 0, stream>>>(hbuf, wg, wu, hh);
  k_down<<<dim3(8, 7), 512, 0, stream>>>(hh, wd, hbuf, (u16*)d_out);
}
```

```cpp
#include <hip/hip_runtime.h>
#include <stdint.h>

typedef unsigned v4u __attribute__((ext_vector_type(4)));
typedef unsigned v2u __attribute__((ext_vector_type(2)));
typedef float f32x4 __attribute__((ext_vector_type(4)));
typedef float f32x2 __attribute__((ext_vector_type(2)));
typedef short bf16x8 __attribute__((ext_vector_type(8)));
typedef short s16x4 __attribute__((ext_vector_type(4)));
typedef __bf16 bf16x2_t __attribute__((ext_vector_type(2)));
typedef unsigned short u16;
typedef unsigned long long u64;
#define LAS __attribute__((address_space(3)))

#define NEXTRA 0
#define KD 1
#define VD 1
#define DMAD 40
#define DMAPOL " nt"
#define WQAUX 0
#define EARLY 64
#define SKEW 0

constexpr int DM = 448, SK = 8192, FF = 1792, NQ = 4096;
constexpr unsigned WS_E = 0, WS_CF = 4096, WS_ML = 8192, WS_P2 = 16384, WS_QG = 262144, WS_P1 = 524288, WS_P3 = 1572864;
constexpr unsigned WS_END = WS_P3 + 4 * 56 * 896 * 8;
constexpr unsigned WS_STAMP = 4u << 20;
constexpr int L_WO = 0, L_GU = 65536, L_WD = 122880, L_MRG = 151552, L_OA = 159744, L_END = 163840;
constexpr int L_WQ = L_WD, L_XN = L_WD + 14336, XN_STRIDE = 912, L_Q = L_OA + 2048, L_TAG = L_OA + 3072;
constexpr int L_HN = L_MRG, L_HRES = L_MRG + 2048, L_HH = L_MRG + 4096;
constexpr int SPIN_CAP = 1 << 18;

__device__ __forceinline__ float lo2f(unsigned u) { return __builtin_bit_cast(float, u << 16); }
__device__ __forceinline__ float hi2f(unsigned u) { return __builtin_bit_cast(float, u & 0xffff0000u); }
__device__ __forceinline__ unsigned cvtpk(float lo, float hi) {
  return __builtin_bit_cast(unsigned, __builtin_convertvector(f32x2{lo, hi}, bf16x2_t));
}
__device__ __forceinline__ float rbf(float x) { return __builtin_bit_cast(float, cvtpk(x, x) << 16); }
__device__ __forceinline__ float u2f(unsigned u) { return __builtin_bit_cast(float, u); }
__device__ __forceinline__ unsigned f2u(float f) { return __builtin_bit_cast(unsigned, f); }
__device__ __forceinline__ f32x4 mfma(v4u a, v4u b, f32x4 c) {
  return __builtin_amdgcn_mfma_f32_16x16x32_bf16(__builtin_bit_cast(bf16x8, a), __builtin_bit_cast(bf16x8, b), c, 0, 0, 0);
}
template <int CTRL> __device__ __forceinline__ float dpp(float x) {
  return __builtin_bit_cast(float, __builtin_amdgcn_mov_dpp(__builtin_bit_cast(int, x), CTRL, 0xf, 0xf, true));
}
template <int CTRL> __device__ __forceinline__ unsigned dppu(unsigned x) {
  return (unsigned)__builtin_amdgcn_mov_dpp((int)x, CTRL, 0xf, 0xf, true);
}
constexpr int XOR1 = 0xB1, XOR2 = 0x4E, HMIR = 0x141, RMIR = 0x140, ROR8 = 0x128;
__device__ __forceinline__ float xrow_max(float x) {
  auto s = __builtin_amdgcn_permlane16_swap(f2u(x), f2u(x), false, false);
  x = fmaxf(u2f(s[0]), u2f(s[1]));
  auto t = __builtin_amdgcn_permlane32_swap(f2u(x), f2u(x), false, false);
  return fmaxf(u2f(t[0]), u2f(t[1]));
}
__device__ __forceinline__ float xrow_sum(float x) {
  auto s = __builtin_amdgcn_permlane16_swap(f2u(x), f2u(x), false, false);
  x = u2f(s[0]) + u2f(s[1]);
  auto t = __builtin_amdgcn_permlane32_swap(f2u(x), f2u(x), false, false);
  return u2f(t[0]) + u2f(t[1]);
}
__device__ __forceinline__ float wave_sum(float x) {
  x += dpp<XOR1>(x); x += dpp<XOR2>(x); x += dpp<HMIR>(x); x += dpp<RMIR>(x);
  return xrow_sum(x);
}
__device__ __forceinline__ v4u ln8(const float (&v)[8], bool act) {
  float s = ((v[0] + v[1]) + (v[2] + v[3])) + ((v[4] + v[5]) + (v[6] + v[7]));
  s = act ? s : 0.f;
  const float mean = wave_sum(s) * (1.0f / DM);
  float q = 0.f;
#pragma unroll
  for (int i = 0; i < 8; ++i) { const float d = v[i] - mean; q = fmaf(d, d, q); }
  q = act ? q : 0.f;
  const float var = rbf(wave_sum(q) * (1.0f / DM));
  const float mb = rbf(mean);
  const float sd = rbf(__builtin_amdgcn_sqrtf(rbf(var + 1e-5f)));
  const float inv = __builtin_amdgcn_rcpf(sd);
  v4u o;
#pragma unroll
  for (int i = 0; i < 4; ++i) {
    const unsigned d = cvtpk(v[2 * i] - mb, v[2 * i + 1] - mb);
    o[i] = cvtpk(lo2f(d) * inv, hi2f(d) * inv);
  }
  return o;
}
__device__ __forceinline__ v4u ln8b(const float (&v)[8], bool more) {
  float s = ((v[0] + v[1]) + (v[2] + v[3])) + ((v[4] + v[5]) + (more ? v[6] + v[7] : 0.f));
  const float mean = wave_sum(s) * (1.0f / DM);
  float q = 0.f, q2 = 0.f;
#pragma unroll
  for (int i = 0; i < 6; ++i) { const float d = v[i] - mean; q = fmaf(d, d, q); }
  { const float d = v[6] - mean, e = v[7] - mean; q2 = fmaf(d, d, e * e); }
  q += more ? q2 : 0.f;
  const float var = rbf(wave_sum(q) * (1.0f / DM));
  const float mb = rbf(mean);
  const float sd = rbf(__builtin_amdgcn_sqrtf(rbf(var + 1e-5f)));
  const float inv = __builtin_amdgcn_rcpf(sd);
  v4u o;
#pragma unroll
  for (int i = 0; i < 4; ++i) {
    const unsigned d = cvtpk(v[2 * i] - mb, v[2 * i + 1] - mb);
    o[i] = cvtpk(lo2f(d) * inv, hi2f(d) * inv);
  }
  return o;
}
__device__ __forceinline__ v4u ld_dev(__amdgpu_buffer_rsrc_t r, unsigned off) { return __builtin_amdgcn_raw_buffer_load_b128(r, off, 0, 16); }
__device__ __forceinline__ void st_dev(__amdgpu_buffer_rsrc_t r, unsigned off, v4u v) { __builtin_amdgcn_raw_buffer_store_b128(v, r, off, 0, 16); }
__device__ __forceinline__ bool all_ok(bool ok) { return __builtin_amdgcn_ballot_w64(!ok) == 0ull; }
__device__ __forceinline__ void glds16(const void* g, unsigned lds_base) {
  unsigned keep;
  asm volatile("s_mov_b32 %0, m0\n\ts_mov_b32 m0, %2\n\ts_nop 0\n\tglobal_load_lds_dwordx4 %1, off" DMAPOL "\n\ts_mov_b32 m0, %0"
               : "=&s"(keep) : "v"(g), "s"(lds_base) : "memory");
}
template <int N, class F> __device__ __forceinline__ void gather(__amdgpu_buffer_rsrc_t r, F off, unsigned ta, unsigned tb, v4u (&v)[N]) {
  unsigned pend = (1u << N) - 1u;
  for (int spin = 0; spin < SPIN_CAP; ++spin) {
#pragma unroll
    for (int i = 0; i < N; ++i)
      if ((pend >> i) & 1u) v[i] = ld_dev(r, off(i));
    unsigned bad = 0u;
#pragma unroll
    for (int i = 0; i < N; ++i) bad |= (v[i][1] ^ ta) | (v[i][3] ^ tb);
    if (all_ok(bad == 0u)) break;
#pragma unroll
    for (int i = 0; i < N; ++i)
      if (all_ok(v[i][1] == ta && v[i][3] == tb)) pend &= ~(1u << i);
  }
}
#define BAR() asm volatile("s_waitcnt lgkmcnt(0)\n\ts_barrier" ::: "memory")
#define SB() __builtin_amdgcn_sched_barrier(0)
#define TS(i) do { } while (0)
#define TSOUT() do { } while (0)

__global__ void __launch_bounds__(512, 2)
fused(const u16* __restrict__ x, const char* __restrict__ K, const char* __restrict__ V, const u16* __restrict__ wq,
      const u16* __restrict__ wo, const u16* __restrict__ wg, const u16* __restrict__ wu, const u16* __restrict__ wd,
      u16* __restrict__ out, char* ws) {
  __shared__ __attribute__((aligned(16))) char lds[L_END];
  LAS char* const L = (LAS char*)lds;
  const unsigned lbase = (unsigned)(uintptr_t)L;
#define LD128(off) (*(LAS v4u*)(L + (off)))
#define LD64(off) (*(LAS v2u*)(L + (off)))
#define LD32(off) (*(LAS unsigned*)(L + (off)))
#define TR(off) __builtin_bit_cast(v2u, __builtin_amdgcn_ds_read_tr16_b64_v4i16((LAS s16x4*)(L + (off))))
  const int tid = threadIdx.x, lane = tid & 63, w = __builtin_amdgcn_readfirstlane(tid >> 6);
  const int bid = blockIdx.x, xc = bid & 7, j = (bid >> 3) & 7, team = bid >> 6;
  const int g = lane >> 4, r = lane & 15;
  const int gid = 2 * j + (xc >> 2), sp = xc & 3, b = 2 * team + (gid >> 3), kvh = gid & 7;
  const int aidx = (b * 8 + kvh) * 4 + sp;
  const int qs = xc * 32 + (bid >> 3);
  const int kq = xc >> 1, ns = (xc & 1) * 7 + j;
  const int fs = xc * 7 + j;
  const __amdgpu_buffer_rsrc_t rw = __builtin_amdgcn_make_buffer_rsrc(ws, 0, WS_END, 0x00020000);
  const bool act = lane < 56;
  const int xl = act ? lane : 55;
  TS(0);

  unsigned tag;
  f32x4 o0 = {0, 0, 0, 0}, o1 = {0, 0, 0, 0};
  v4u xres = {0, 0, 0, 0};

  if (w < 4) {
    const int nst = 64 + ((sp & 1) ? -SKEW : SKEW);
    const int beg = (sp >> 1) * 512 + ((sp & 1) ? 256 + 4 * SKEW : 0);
    const size_t base = ((size_t)(b * 8 + kvh) * 1024 + beg) * 4096;
    const int koff = (r >> 2) * 512 + g * 64 + (r & 3) * 16, voff = g * 512 + r * 16;
    v4u kb[KD][4], vb[VD][4], ke[NEXTRA + 1][4], ve[NEXTRA + 1][4];
#define ISSUE(BUF, PTR, OFF, N) do { const bool ok_ = (N) < nst; const int so_ = ok_ ? ((N) * 4 + w) * 4096 : 0; \
      const __amdgpu_buffer_rsrc_t r_ = __builtin_amdgcn_make_buffer_rsrc((void*)(PTR + base), 0, ok_ ? nst * 16384 : 0, 0x00020000); \
      _Pragma("unroll") for (int i = 0; i < 4; ++i) BUF[i] = __builtin_amdgcn_raw_buffer_load_b128(r_, OFF + (i >> 1) * 2048 + (i & 1) * 256, so_, 2); } while (0)
    v4u xv = *(const v4u*)(x + w * DM + xl * 8), xw = *(const v4u*)(x + (w + 4) * DM + xl * 8);
    SB();
    asm volatile("s_barrier" ::: "memory");
    SB();
#pragma unroll
    for (int s = 0; s < 2; ++s) { if (s < KD) ISSUE(kb[s], K, koff, NEXTRA + s); if (s < VD) ISSUE(vb[s], V, voff, NEXTRA + s); }
#pragma unroll
    for (int s = 0; s < NEXTRA; ++s) { ISSUE(ke[s], K, koff, s); ISSUE(ve[s], V, voff, s); }
    SB();
    asm volatile("" : "+v"(xv), "+v"(xw));
    {
      float v[8], u[8];
#pragma unroll
      for (int i = 0; i < 4; ++i) { v[2 * i] = lo2f(xv[i]); v[2 * i + 1] = hi2f(xv[i]); u[2 * i] = lo2f(xw[i]); u[2 * i + 1] = hi2f(xw[i]); }
      const v4u xn = ln8(v, act), xm = ln8(u, act);
      if (act) { LD128(L_XN + w * XN_STRIDE + lane * 16) = xn; LD128(L_XN + (w + 4) * XN_STRIDE + lane * 16) = xm; }
    }
    const int h = r >> 3, etal = (r >> 2) & 1, sig = r & 3;
    unsigned msk[4];
#pragma unroll
    for (int i = 0; i < 4; ++i) msk[i] = (((lane & 7) >> 1) == i) ? ((lane & 1) ? 0xffff0000u : 0x0000ffffu) : 0u;
    unsigned m1 = (sig & 1) ? ~0u : 0u, m2 = (sig & 2) ? ~0u : 0u;
    asm volatile("" : "+v"(m1), "+v"(m2));
    SB();
    BAR();
    BAR();
    SB();
    TS(1);
    v4u qb[2][2];
    {
      const v4u q0 = LD128(L_Q + (h * 256 + 32 * g + 8 * sig) * 2), q1 = LD128(L_Q + (h * 256 + 128 + 32 * g + 8 * sig) * 2);
      const v4u z = {0, 0, 0, 0};
      qb[0][0] = etal == 0 ? q0 : z; qb[0][1] = etal == 1 ? q0 : z;
      qb[1][0] = etal == 0 ? q1 : z; qb[1][1] = etal == 1 ? q1 : z;
    }
    tag = __builtin_amdgcn_readfirstlane(LD32(L_TAG));
    float m = -INFINITY, l = 0.f;
    constexpr float C2 = 0.0625f * 1.4426950408889634f;
#define STAGE(KB, VB, N, DO_ISSUE) do { \
      f32x4 d = {0, 0, 0, 0}; \
      d = mfma(KB[0], qb[0][0], d); d = mfma(KB[1], qb[1][0], d); d = mfma(KB[2], qb[0][1], d); d = mfma(KB[3], qb[1][1], d); \
      asm volatile("" : "+v"(d)); \
      SB(); \
      if (DO_ISSUE) ISSUE(KB, K, koff, (N) + KD); \
      SB(); \
      const unsigned sl = (f2u(d[1]) & m1) | (f2u(d[0]) & ~m1), sh = (f2u(d[3]) & m1) | (f2u(d[2]) & ~m1); \
      float sc = u2f((sh & m2) | (sl & ~m2)); \
      sc += dpp<XOR1>(sc); sc += dpp<XOR2>(sc); \
      sc = rbf(sc) * C2; \
      float pm = fmaxf(sc, dpp<HMIR>(sc)); pm = xrow_max(pm); \
      const float mn = fmaxf(m, pm); \
      const float alpha = __builtin_amdgcn_exp2f(m - mn); \
      const float p = __builtin_amdgcn_exp2f(sc - mn); \
      m = mn; l = fmaf(l, alpha, p); \
      o0 *= alpha; o1 *= alpha; \
      const unsigned pw = cvtpk(p, p), pwo = dppu<HMIR>(pw); \
      const v4u b0 = {pw & msk[0], pw & msk[1], pwo & msk[2], pwo & msk[3]}; \
      const v4u b1 = {pwo & msk[0], pwo & msk[1], pw & msk[2], pw & msk[3]}; \
      o0 = mfma(VB[0], b0, o0); o1 = mfma(VB[1], b0, o1); o0 = mfma(VB[2], b1, o0); o1 = mfma(VB[3], b1, o1); \
      asm volatile("" : "+v"(o0), "+v"(o1)); \
      SB(); \
      if (DO_ISSUE) ISSUE(VB, V, voff, (N) + VD); \
      SB(); \
    } while (0)
    static_assert(((64 - NEXTRA + SKEW) & 1) == 0, "rotating stages come in pairs");
    STAGE(kb[0], vb[0], NEXTRA, true);
    STAGE(kb[1 % KD], vb[1 % VD], NEXTRA + 1, true);
#pragma unroll
    for (int s = 0; s < NEXTRA; ++s) STAGE(ke[s], ve[s], 0, false);
    for (int n = NEXTRA + 2; n < nst; n += 2) {
      STAGE(kb[0], vb[0], n, true);
      STAGE(kb[1 % KD], vb[1 % VD], n + 1, true);
    }
    l = l + dpp<HMIR>(l); l = xrow_sum(l);
    LD128(L_MRG + w * 2048 + lane * 16) = __builtin_bit_cast(v4u, o0);
    LD128(L_MRG + w * 2048 + 1024 + lane * 16) = __builtin_bit_cast(v4u, o1);
    if (g == 0 && (lane & 4) == 0) LD64(L_OA + (lane & 3) * 512 + w * 16 + h * 8) = v2u{f2u(m), f2u(l)};
    TS(2);
  } else {
    if (w == 4) {
      const v2u E = __builtin_amdgcn_raw_buffer_load_b64(rw, WS_E, 0, 0);
      const u16* src = wq + (size_t)(4 * ((lane >> 3) ^ ((lane >> 4) & 1)) + ((lane >> 1) & 3)) * NQ + qs * 16 + (lane & 1) * 8;
#pragma unroll
      for (int I = 0; I < 14; ++I) glds16(src + (size_t)I * 32 * NQ, lbase + L_WQ + I * 1024);
      SB();
      asm volatile("s_barrier" ::: "memory");
      asm volatile("s_waitcnt vmcnt(0)" ::: "memory");
      BAR();
      TS(1);
      tag = __builtin_amdgcn_readfirstlane(E[0]) + 1u;
      const unsigned tagb = __builtin_amdgcn_readfirstlane(E[1]) + (tag == 0u ? 1u : 0u);
      f32x4 d0 = {0, 0, 0, 0}, d1 = {0, 0, 0, 0};
      const int a0 = L_WQ + (2 * g + (g & 1)) * 128 + r * 8, a1 = L_WQ + (2 * g + 1 - (g & 1)) * 128 + r * 8;
      const int bo = L_XN + (r & 7) * XN_STRIDE + g * 16;
#pragma unroll
      for (int ks = 0; ks < 14; ++ks) {
        const v2u t0 = TR(a0 + ks * 1024), t1 = TR(a1 + ks * 1024);
        const v4u a = {t0[0], t0[1], t1[0], t1[1]};
        const v4u bb = LD128(bo + ks * 64);
        if (ks & 1) d1 = mfma(a, bb, d1); else d0 = mfma(a, bb, d0);
      }
      d0 += d1;
      const unsigned pk = (r & 8) ? cvtpk(d0[2], d0[3]) : cvtpk(d0[0], d0[1]);
      st_dev(rw, WS_QG + ((r & 7) * 2048 + qs * 8 + 2 * g + (r >> 3)) * 16, v4u{pk, tag, pk, tagb});
      TS(2);
      v4u qv[4];
      const unsigned qoff = WS_QG + (b * 2048 + kvh * 256 + lane) * 16;
      gather<4>(rw, [&](int i) { return qoff + i * 1024; }, tag, tagb, qv);
#pragma unroll
      for (int i = 0; i < 4; ++i) LD32(L_Q + (64 * i + lane) * 4) = qv[i][0];
      if (lane == 0) LD32(L_TAG) = tag;
      BAR();
      TS(3);
      const v4u c = {0u, ~tag, 0u, ~tag};
#pragma unroll
      for (int i = 0; i < 4; ++i) st_dev(rw, WS_P1 + aidx * 4096 + i * 1024 + lane * 16, c);
      if (lane < 2) st_dev(rw, WS_ML + aidx * 32 + lane * 16, c);
      if (j < 7) {
        if (lane < 32) st_dev(rw, WS_P2 + (((team * 2 + (lane >> 4)) * 4 + kq) * DM + ns * 32 + (lane & 15) * 2) * 8, c);
#pragma unroll
        for (int i = 0; i < 7; ++i) st_dev(rw, WS_P3 + (team * 56 + fs) * 7168 + i * 1024 + lane * 16, c);
      }
      asm volatile("s_waitcnt vmcnt(0)" ::: "memory");
      if (lane == 0) __builtin_amdgcn_raw_buffer_store_b64(v2u{tag, tagb}, rw, WS_CF + bid * 8, 0, 16);
      const int nchk = (bid == 56) ? 4 : 1;
      for (int i = 0; i < nchk; ++i) {
        const unsigned off = WS_CF + (((team + i) & 3) * 64 + lane) * 8;
#pragma nounroll
        for (int spin = 0; spin < SPIN_CAP; ++spin) {
          const v2u f = __builtin_amdgcn_raw_buffer_load_b64(rw, off, 0, 16);
          if (all_ok(f[0] == tag && f[1] == tagb)) break;
          __builtin_amdgcn_s_sleep(32);
        }
      }
      if (bid == 56 && lane == 0) __builtin_amdgcn_raw_buffer_store_b64(v2u{tag, tagb}, rw, WS_E, 0, 16);
    } else {
      const int e4 = (lane >> 1) & 3, hf = lane & 1;
      const u16* so = wo + (size_t)(kq * 1024 + (e4 & 1) * 8 + (lane >> 5) * 4 + ((lane >> 4) & 1) * 2 + (e4 >> 1)) * DM + ns * 32 + (((lane >> 3) & 1) ^ (lane >> 5)) * 16 + hf * 8;
      const u16* sg = (hf ? wu : wg) + (size_t)(4 * (lane >> 5) + e4) * FF + fs * 32;
#define DMA_WO(I) glds16(so + (((I) >> 3) * 128 + (((I) >> 1) & 1) * 64 + ((I) & 1) * 32 + (((I) >> 2) & 1) * 16) * DM, lbase + L_WO + (I) * 1024)
#define DMA_GU(I) glds16(sg + (size_t)(I) * 8 * FF + (((lane >> 3) & 3) ^ ((I) & 1)) * 8, lbase + L_GU + (I) * 1024)
#define DMAW() asm volatile("s_waitcnt vmcnt(%0)" :: "n"(DMAD) : "memory")
      asm volatile("s_barrier" ::: "memory");
      if (j < 7) {
        for (int I = w - 5; I < EARLY && I < 64; I += 3) DMA_WO(I);
        for (int I = w - 5; I < EARLY - 64; I += 3) DMA_GU(I);
      }
      asm volatile("s_barrier" ::: "memory");
      asm volatile("s_barrier" ::: "memory");
      tag = __builtin_amdgcn_readfirstlane(LD32(L_TAG));
      if (j < 7) {
        for (int I = (EARLY < 64 ? EARLY : 64) + w - 5; I < 64; I += 3) { DMA_WO(I); DMAW(); }
        TS(1);
        for (int I = (EARLY > 64 ? EARLY - 64 : 0) + w - 5; I < 56; I += 3) { DMA_GU(I); DMAW(); }
        TS(2);
        for (int I = w - 5; I < 28; I += 3) {
          const int pos = 8 * I + (lane >> 3), k4 = pos / 28, tp = pos - 28 * k4;
          glds16(wd + (size_t)(fs * 32 + 4 * k4 + e4) * DM + (tp ^ ((k4 >> 1) & 1)) * 16 + hf * 8, lbase + L_WD + I * 1024);
          DMAW();
        }
        if (w < 7) {
          const u16* xr = x + (2 * team + (w - 5)) * DM + lane * 2;
#pragma unroll
          for (int i = 0; i < 4; ++i) xres[i] = *(const unsigned*)(xr + ((i < 3 || lane < 32) ? i * 128 : 0));
        }
      }
    }
    asm volatile("s_waitcnt vmcnt(0)" ::: "memory");
    TS(4);
  }
  tag = __builtin_amdgcn_readfirstlane(tag);
  BAR();
  TS(5);
  if (w < 4) {
    const int h = (lane >> 3) & 1;
    v2u pv[4], ml[4];
#pragma unroll
    for (int i = 0; i < 4; ++i) { pv[i] = LD64(L_MRG + i * 2048 + (w >> 1) * 1024 + lane * 16 + (w & 1) * 8); ml[i] = LD64(L_OA + w * 512 + i * 16 + h * 8); }
    const float M = fmaxf(fmaxf(u2f(ml[0][0]), u2f(ml[1][0])), fmaxf(u2f(ml[2][0]), u2f(ml[3][0])));
    float a0 = 0.f, a1 = 0.f, Ls = 0.f;
#pragma unroll
    for (int i = 0; i < 4; ++i) {
      const float f = __builtin_amdgcn_exp2f(u2f(ml[i][0]) - M);
      a0 = fmaf(f, u2f(pv[i][0]), a0); a1 = fmaf(f, u2f(pv[i][1]), a1); Ls = fmaf(f, u2f(ml[i][1]), Ls);
    }
    st_dev(rw, WS_P1 + aidx * 4096 + w * 1024 + lane * 16, v4u{f2u(a0), tag, f2u(a1), tag});
    if (w == 0 && (lane & 0x37) == 0) st_dev(rw, WS_ML + aidx * 32 + h * 16, v4u{f2u(M), tag, f2u(Ls), tag});
  }
  if (j == 7) { TSOUT(); return; }
  TS(6);
  {
    v4u wa[4][2];
#pragma unroll
    for (int s = 0; s < 4; ++s)
#pragma unroll
      for (int c = 0; c < 2; ++c) {
        const int a = L_WO + (4 * w + s) * 2048 + g * 512 + (c ^ (g & 1)) * 128 + r * 8;
        const v2u t0 = TR(a), t1 = TR(a + 256);
        wa[s][c] = v4u{t0[0], t0[1], t1[0], t1[1]};
      }
    const int a0 = ((2 * team) * 8 + 2 * kq + (w >> 2)) * 4, hh = (w >> 1) & 1;
    const unsigned doff = WS_P1 + a0 * 4096 + (w & 1) * 2048 + (lane >> 5) * 1024 + (((lane >> 3) & 3) * 16 + hh * 8 + (lane & 7)) * 16;
    const unsigned moff = WS_ML + (a0 + ((lane >> 2) & 1) * 32 + (lane & 3)) * 32 + hh * 16;
    v4u v[8], ml = {0, 0, 0, 0};
    unsigned pend = 0xffu, seen = 0u, want = 0xffu;
    for (int spin = 0; spin < SPIN_CAP; ++spin) {
      if (seen != 0xffu) ml = ld_dev(rw, moff);
#pragma unroll
      for (int i = 0; i < 8; ++i)
        if ((want >> i) & 1u) v[i] = ld_dev(rw, doff + (i >> 2) * (32 * 4096) + (i & 3) * 4096);
      unsigned bad = (ml[1] ^ tag) | (ml[3] ^ tag);
#pragma unroll
      for (int i = 0; i < 8; ++i) bad |= (v[i][1] ^ tag) | (v[i][3] ^ tag);
      if (all_ok(bad == 0u)) break;
      seen = (unsigned)__builtin_amdgcn_ballot_w64(ml[1] == tag && ml[3] == tag) & 0xffu;
#pragma unroll
      for (int i = 0; i < 8; ++i)
        if (all_ok(v[i][1] == tag && v[i][3] == tag)) pend &= ~(1u << i);
      want = (__builtin_popcount(pend) <= 2) ? pend : (pend & seen);
      if (want == 0u) __builtin_amdgcn_s_sleep(4);
    }
    TS(7);
    float M = fmaxf(u2f(ml[0]), dpp<XOR1>(u2f(ml[0]))); M = fmaxf(M, dpp<XOR2>(M));
    const float f = __builtin_amdgcn_exp2f(u2f(ml[0]) - M);
    float Ls = f * u2f(ml[2]); Ls += dpp<XOR1>(Ls); Ls += dpp<XOR2>(Ls);
    const float cf = f * __builtin_amdgcn_rcpf(Ls);
#pragma unroll
    for (int b2 = 0; b2 < 2; ++b2) {
      float a0s = 0.f, a1s = 0.f;
#pragma unroll
      for (int s = 0; s < 4; ++s) {
        const float c = u2f(__builtin_amdgcn_readlane(f2u(cf), b2 * 4 + s));
        a0s = fmaf(c, u2f(v[b2 * 4 + s][0]), a0s); a1s = fmaf(c, u2f(v[b2 * 4 + s][2]), a1s);
      }
      LD32(L_OA + w * 512 + b2 * 256 + lane * 4) = cvtpk(a0s, a1s);
    }
    f32x4 d0 = {0, 0, 0, 0}, d1 = {0, 0, 0, 0};
#pragma unroll
    for (int s = 0; s < 4; ++s) {
      const v4u bb = LD128(L_OA + w * 512 + (r & 1) * 256 + s * 64 + g * 16);
      d0 = mfma(wa[s][0], bb, d0); d1 = mfma(wa[s][1], bb, d1);
    }
    const f32x4 ds = (r & 2) ? d1 : d0;
    if (r < 4) LD128(L_OA + w * 512 + (r & 1) * 128 + (r >> 1) * 64 + g * 16) = __builtin_bit_cast(v4u, ds);
  }
  BAR();
  TS(8);
  v4u ga[14][1];
  v4u da[4];
  if (w == 4) {
    if (lane < 32) {
      float a0 = 0.f, a1 = 0.f;
#pragma unroll
      for (int i = 0; i < 8; ++i) { const v2u t = LD64(L_OA + i * 512 + lane * 8); a0 += u2f(t[0]); a1 += u2f(t[1]); }
      st_dev(rw, WS_P2 + (((team * 2 + (lane >> 4)) * 4 + kq) * DM + ns * 32 + (lane & 15) * 2) * 8, v4u{f2u(a0), tag, f2u(a1), tag});
    }
  } else if (w == 5 || w == 6) {
    v4u v[16];
    const unsigned poff = WS_P2 + (team * 2 + (w - 5)) * (4 * DM * 8) + lane * 16, p3 = lane < 32 ? 3072u : 0u;
    gather<16>(rw, [&](int i) { return poff + (i >> 2) * (DM * 8) + ((i & 3) < 3 ? (i & 3) * 1024u : p3); }, tag, tag, v);
    TS(9);
    float hv[8];
#pragma unroll
    for (int i = 0; i < 4; ++i) {
      const float s0 = (u2f(v[i][0]) + u2f(v[4 + i][0])) + (u2f(v[8 + i][0]) + u2f(v[12 + i][0]));
      const float s1 = (u2f(v[i][2]) + u2f(v[4 + i][2])) + (u2f(v[8 + i][2]) + u2f(v[12 + i][2]));
      const unsigned at = cvtpk(s0, s1);
      xres[i] = cvtpk(lo2f(xres[i]) + lo2f(at), hi2f(xres[i]) + hi2f(at));
      hv[2 * i] = lo2f(xres[i]); hv[2 * i + 1] = hi2f(xres[i]);
    }
    const v4u hn = ln8b(hv, lane < 32);
#pragma unroll
    for (int i = 0; i < 4; ++i)
      if (i < 3 || lane < 32) { LD32(L_HN + (w - 5) * XN_STRIDE + i * 256 + lane * 4) = hn[i]; LD32(L_HRES + (w - 5) * (DM * 2) + i * 256 + lane * 4) = xres[i]; }
  } else if (w < 4) {
#pragma unroll
    for (int ks = 0; ks < 14; ++ks) {
      const int a = L_GU + ks * 4096 + g * 1024 + (w ^ (g & 1)) * 128 + r * 8;
      const v2u t0 = TR(a), t1 = TR(a + 512);
      ga[ks][0] = v4u{t0[0], t0[1], t1[0], t1[1]};
    }
  }
  if (w < 7) {
#pragma unroll
    for (int t = 0; t < 4; ++t) {
      const int a = L_WD + (2 * g * 28 + ((4 * w + t) ^ (g & 1))) * 128 + r * 8;
      const v2u t0 = TR(a), t1 = TR(a + 28 * 128);
      da[t] = v4u{t0[0], t0[1], t1[0], t1[1]};
    }
  }
  BAR();
  TS(10);
  if (w < 4) {
    f32x4 d0 = {0, 0, 0, 0}, d1 = {0, 0, 0, 0};
#pragma unroll
    for (int ks = 0; ks < 14; ++ks) {
      const v4u bb = LD128(L_HN + (r & 1) * XN_STRIDE + ks * 64 + g * 16);
      if (ks & 1) d1 = mfma(ga[ks][0], bb, d1); else d0 = mfma(ga[ks][0], bb, d0);
    }
    d0 += d1;
    const unsigned p0 = cvtpk(d0[0], d0[1]), p1 = cvtpk(d0[2], d0[3]);
    auto s0 = __builtin_amdgcn_permlane32_swap(p0, p0, false, false);
    auto s1 = __builtin_amdgcn_permlane32_swap(p1, p1, false, false);
    const unsigned h0 = cvtpk(lo2f(s0[0]) * lo2f(s0[1]), hi2f(s0[0]) * hi2f(s0[1]));
    const unsigned h1 = cvtpk(lo2f(s1[0]) * lo2f(s1[1]), hi2f(s1[0]) * hi2f(s1[1]));
    if (g < 2 && r < 2) LD64(L_HH + r * 64 + w * 16 + g * 8) = v2u{h0, h1};
  }
  BAR();
  TS(11);
  if (w < 7) {
    const v4u bb = LD128(L_HH + (r & 1) * 64 + g * 16);
    const f32x4 z = {0, 0, 0, 0};
    const f32x4 e0 = mfma(da[0], bb, z), e1 = mfma(da[1], bb, z), e2 = mfma(da[2], bb, z), e3 = mfma(da[3], bb, z);
    const f32x4 e = (r & 8) ? ((r & 4) ? e3 : e2) : ((r & 4) ? e1 : e0);
    const float y0 = (r & 2) ? e[2] : e[0], y1 = (r & 2) ? e[3] : e[1];
    st_dev(rw, WS_P3 + (team * 56 + fs) * 7168 + ((r & 1) * DM + 16 * (4 * w + (r >> 2)) + 4 * g + (r & 2)) * 8, v4u{f2u(y0), tag, f2u(y1), tag});
    TS(12);
  } else {
    const unsigned hr = LD32(L_HRES + fs * 32 + (lane & 7) * 4);
    v4u v[7];
    const unsigned foff = WS_P3 + (team * 56 + (lane >> 3) * 7) * 7168 + fs * 128 + (lane & 7) * 16;
    gather<7>(rw, [&](int i) { return foff + i * 7168; }, tag, tag, v);
    float s0 = 0.f, s1 = 0.f;
#pragma unroll
    for (int i = 0; i < 7; ++i) { s0 += u2f(v[i][0]); s1 += u2f(v[i][2]); }
    s0 += dpp<ROR8>(s0); s1 += dpp<ROR8>(s1);
    s0 = xrow_sum(s0); s1 = xrow_sum(s1);
    const unsigned y = cvtpk(s0, s1);
    if (lane < 8) *(unsigned*)(out + team * 896 + fs * 16 + lane * 2) = cvtpk(lo2f(hr) + lo2f(y), hi2f(hr) + hi2f(y));
    TS(12);
  }
  TSOUT();
}

extern "C" void kernel_launch(void* const* d_in, const int* in_sizes, int n_in, void* d_out, int out_size, void* d_ws, size_t ws_size,
                              hipStream_t stream) {
  fused<<<256, 512, 0, stream>>>((const u16*)d_in[0], (const char*)d_in[1], (const char*)d_in[2], (const u16*)d_in[3], (const u16*)d_in[4],
                                 (const u16*)d_in[6], (const u16*)d_in[7], (const u16*)d_in[8], (u16*)d_out, (char*)d_ws);
}
```

```cpp
#include <hip/hip_runtime.h>
#include <stdint.h>

typedef unsigned v4u __attribute__((ext_vector_type(4)));
typedef unsigned v2u __attribute__((ext_vector_type(2)));
typedef float f32x4 __attribute__((ext_vector_type(4)));
typedef float f32x2 __attribute__((ext_vector_type(2)));
typedef short bf16x8 __attribute__((ext_vector_type(8)));
typedef short s16x4 __attribute__((ext_vector_type(4)));
typedef __bf16 bf16x2_t __attribute__((ext_vector_type(2)));
typedef unsigned short u16;
typedef unsigned long long u64;
#define LAS __attribute__((address_space(3)))

constexpr int TOFF = 100, TEND = 7200, C0 = 1150, CS = 650, NK = 8, SW = 60, PT = 300;
constexpr float FL = -35.f, X0 = 2.f, XC = 12.f, XS = 80.f, GR = 4.f, JC = 4.f, HD = 0.7f, DMX = 800.f;
constexpr int GQD = 30, H1T = 50, TD = 1000, DT = 38, CFS = 8;
constexpr int GS = 32, EX = 8, WV = 8;
constexpr int DM = 448, FF = 1792, NQ = 4096;
constexpr int NC = 4;
constexpr unsigned WS_E = 0, WS_ML = 8192, WS_RS = 16384, WS_CF = 24576, WS_PB = 32768, WS_QG = 64u << 10, WS_P1 = 1088u << 10, WS_P2 = 1600u << 10,
                   WS_P3 = 2u << 20;
constexpr unsigned P2C = 4 * 2 * 4 * DM * 4;
constexpr unsigned WS_END = WS_P3 + 224 * 3584;
constexpr unsigned OOR = 0x40000000u;
static_assert(WS_P2 + NC * P2C <= WS_P3, "");
constexpr int L_WO = 0, L_GU = 65536, L_WD = 122880, L_MRG = 151552, L_OA = 159744, L_END = 163840;
constexpr int L_WQ = L_WD, L_XN = L_WD + 16384, XN_STRIDE = 912, L_Q = L_OA + 2048, L_FLG = L_OA + 3072;
constexpr int L_HN = L_MRG, L_HRES = L_MRG + 2048, L_HH = L_MRG + 4096;
constexpr int SPIN_CAP = 1 << 18;
constexpr unsigned SENT = 0xffffffffu;

__device__ __forceinline__ float lo2f(unsigned u) { return __builtin_bit_cast(float, u << 16); }
__device__ __forceinline__ float hi2f(unsigned u) { return __builtin_bit_cast(float, u & 0xffff0000u); }
__device__ __forceinline__ unsigned cvtpk(float lo, float hi) {
  return __builtin_bit_cast(unsigned, __builtin_convertvector(f32x2{lo, hi}, bf16x2_t));
}
__device__ __forceinline__ float rbf(float x) { return __builtin_bit_cast(float, cvtpk(x, x) << 16); }
__device__ __forceinline__ float u2f(unsigned u) { return __builtin_bit_cast(float, u); }
__device__ __forceinline__ unsigned f2u(float f) { return __builtin_bit_cast(unsigned, f); }
__device__ __forceinline__ unsigned umax(unsigned a, unsigned b) { return a > b ? a : b; }
__device__ __forceinline__ unsigned word(float f) { const unsigned u = f2u(f); return u < SENT ? u : SENT - 1u; }
__device__ __forceinline__ f32x4 mfma(v4u a, v4u b, f32x4 c) {
  return __builtin_amdgcn_mfma_f32_16x16x32_bf16(__builtin_bit_cast(bf16x8, a), __builtin_bit_cast(bf16x8, b), c, 0, 0, 0);
}
template <int CTRL> __device__ __forceinline__ float dpp(float x) {
  return __builtin_bit_cast(float, __builtin_amdgcn_mov_dpp(__builtin_bit_cast(int, x), CTRL, 0xf, 0xf, true));
}
template <int CTRL> __device__ __forceinline__ unsigned dppu(unsigned x) {
  return (unsigned)__builtin_amdgcn_mov_dpp((int)x, CTRL, 0xf, 0xf, true);
}
constexpr int XOR1 = 0xB1, XOR2 = 0x4E, HMIR = 0x141, RMIR = 0x140, ROR4 = 0x124, ROR8 = 0x128, BCAST = 0x150;
__device__ __forceinline__ float xrow_max(float x) {
  auto s = __builtin_amdgcn_permlane16_swap(f2u(x), f2u(x), false, false);
  x = fmaxf(u2f(s[0]), u2f(s[1]));
  auto t = __builtin_amdgcn_permlane32_swap(f2u(x), f2u(x), false, false);
  return fmaxf(u2f(t[0]), u2f(t[1]));
}
__device__ __forceinline__ float xrow_sum(float x) {
  auto s = __builtin_amdgcn_permlane16_swap(f2u(x), f2u(x), false, false);
  x = u2f(s[0]) + u2f(s[1]);
  auto t = __builtin_amdgcn_permlane32_swap(f2u(x), f2u(x), false, false);
  return u2f(t[0]) + u2f(t[1]);
}
__device__ __forceinline__ float wave_sum(float x) {
  x += dpp<XOR1>(x); x += dpp<XOR2>(x); x += dpp<HMIR>(x); x += dpp<RMIR>(x);
  return xrow_sum(x);
}
__device__ __forceinline__ v4u ln8(const float (&v)[8], bool a0, bool a1) {
  const float c = u2f(__builtin_amdgcn_readfirstlane(f2u(v[0])));
  float s0 = 0.f, s1 = 0.f, q0 = 0.f, q1 = 0.f;
#pragma unroll
  for (int i = 0; i < 4; ++i) { const float d = v[i] - c, e = v[4 + i] - c; s0 += d; s1 += e; q0 = fmaf(d, d, q0); q1 = fmaf(e, e, q1); }
  const float ms = wave_sum((a0 ? s0 : 0.f) + (a1 ? s1 : 0.f)) * (1.0f / DM);
  const float mq = wave_sum((a0 ? q0 : 0.f) + (a1 ? q1 : 0.f)) * (1.0f / DM);
  const float mean = c + ms;
  const float var = rbf(fmaxf(fmaf(-ms, ms, mq), 0.f));
  const float mb = rbf(mean);
  const float sd = rbf(__builtin_amdgcn_sqrtf(rbf(var + 1e-5f)));
  const float inv = __builtin_amdgcn_rcpf(sd);
  v4u o;
#pragma unroll
  for (int i = 0; i < 4; ++i) {
    const unsigned d = cvtpk(v[2 * i] - mb, v[2 * i + 1] - mb);
    o[i] = cvtpk(lo2f(d) * inv, hi2f(d) * inv);
  }
  return o;
}
__device__ __forceinline__ v4u ld_dev(__amdgpu_buffer_rsrc_t r, unsigned off) { return __builtin_amdgcn_raw_buffer_load_b128(r, off, 0, 16); }
__device__ __forceinline__ void st_dev(__amdgpu_buffer_rsrc_t r, unsigned off, v4u v) { __builtin_amdgcn_raw_buffer_store_b128(v, r, off, 0, 16); }
__device__ __forceinline__ v2u ld_dev8(__amdgpu_buffer_rsrc_t r, unsigned off) { return __builtin_amdgcn_raw_buffer_load_b64(r, off, 0, 16); }
__device__ __forceinline__ void st_dev8(__amdgpu_buffer_rsrc_t r, unsigned off, v2u v) { __builtin_amdgcn_raw_buffer_store_b64(v, r, off, 0, 16); }
__device__ __forceinline__ bool all_ok(bool ok) { return __builtin_amdgcn_ballot_w64(!ok) == 0ull; }
__device__ __forceinline__ unsigned top(v4u v) { return umax(umax(v[0], v[1]), umax(v[2], v[3])); }
__device__ __forceinline__ v4u words(f32x4 f) { return v4u{word(f[0]), word(f[1]), word(f[2]), word(f[3])}; }
__device__ __forceinline__ void glds16(const void* g, unsigned lds_base) {
  unsigned keep;
  asm volatile("s_mov_b32 %0, m0\n\ts_mov_b32 m0, %2\n\ts_nop 0\n\tglobal_load_lds_dwordx4 %1, off nt\n\ts_mov_b32 m0, %0"
               : "=&s"(keep) : "v"(g), "s"(lds_base) : "memory");
}
template <int N> __device__ __forceinline__ void blank(v4u (&v)[N]) {
#pragma unroll
  for (int i = 0; i < N; ++i) asm volatile("" : "=v"(v[i]));
}
template <int N> __device__ __forceinline__ void gather(__amdgpu_buffer_rsrc_t r, const unsigned (&off)[N], v4u (&v)[N]) {
  for (int spin = 0; spin < SPIN_CAP; ++spin) {
    asm volatile("" ::: "memory");
#pragma unroll
    for (int i = 0; i < N; ++i) v[i] = ld_dev(r, off[i]);
    unsigned tt = 0u;
#pragma unroll
    for (int i = 0; i < N; ++i) tt = umax(umax(tt, v[i][0]), v[i][1]), tt = umax(umax(tt, v[i][2]), v[i][3]);
    if (all_ok(tt != SENT)) break;
  }
}
template <class T> __device__ __forceinline__ T pin(T a) { asm volatile("" : "+v"(a)); return a; }
#define BAR() asm volatile("s_waitcnt lgkmcnt(0)\n\ts_barrier" ::: "memory")
#define SB() __builtin_amdgcn_sched_barrier(0)
#define NOW() ((unsigned)__builtin_amdgcn_s_memrealtime())
#define UNI(x) __builtin_amdgcn_readfirstlane(x)
#define TWAIT(T, SL) do { _Pragma("nounroll") for (int s_ = 0; s_ < 8192; ++s_) { if ((int)(NOW() - (T)) >= 0) break; __builtin_amdgcn_s_sleep(SL); } } while (0)

__global__ void __launch_bounds__(512, 2)
fused(const u16* __restrict__ x, const char* __restrict__ K, const char* __restrict__ V, const u16* __restrict__ wq, char* ws,
      const u16* __restrict__ wo, const u16* __restrict__ wg, const u16* __restrict__ wu, const u16* __restrict__ wd,
      u16* __restrict__ out) {
  __shared__ __attribute__((aligned(16))) char lds[L_END];
  LAS char* const L = (LAS char*)lds;
  const unsigned lbase = (unsigned)(uintptr_t)L;
#define LD128(off) (*(LAS v4u*)(L + (off)))
#define LD64(off) (*(LAS v2u*)(L + (off)))
#define LD32(off) (*(LAS unsigned*)(L + (off)))
#define VLD32(off) (*(volatile LAS unsigned*)(L + (off)))
#define TR(off) __builtin_bit_cast(v2u, __builtin_amdgcn_ds_read_tr16_b64_v4i16((LAS s16x4*)(L + (off))))
#define LWAIT(off) do { for (int s_ = 0; s_ < SPIN_CAP * 16; ++s_) { if (all_ok(VLD32(off) != 0u)) break; __builtin_amdgcn_s_sleep(1); } asm volatile("" ::: "memory"); } while (0)
  const int tid = threadIdx.x, lane = tid & 63, w = UNI(tid >> 6);
  unsigned cpf;
  u64 pc; unsigned len;
  {
    asm volatile("s_getpc_b64 %0\nLcode0:\n\ts_mov_b32 %1, Lcode1-Lcode0" : "=s"(pc), "=s"(len));
    const unsigned o = tid * 64;
    cpf = *(const __attribute__((address_space(1))) unsigned*)(pc + (o < len ? o : len));
    SB();
  }
  const int bid = blockIdx.x, xc = bid & 7, j = (bid >> 3) & 7, team = bid >> 6;
  const int g = lane >> 4, r = lane & 15;
  const int sp = (j & 1) + 2 * (xc & 1), b = 2 * team + (xc >> 2), kvh = (j >> 1) + 2 * (xc & 2);
  const int aidx = (b * 8 + kvh) * 4 + sp;
  const int ql = bid >> 5, qd = (bid >> 3) & 3;
  const int qk0 = qd < 2 ? 128 * qd : 96 * qd + 64;
  const int kq = xc >> 1, ns = (xc & 1) * 7 + j;
  const int fs = xc * 7 + j;
  const __amdgpu_buffer_rsrc_t rw = __builtin_amdgcn_make_buffer_rsrc(ws, 0, WS_END, 0x00020000);
  const unsigned tk0 = NOW();
  v4u xres = {0, 0, 0, 0};
  const int mh = (lane >> 3) & 1;
  int mpv = 0, mml = 0; unsigned mst = 0, msl = 0;

  const size_t base = (size_t)(b * 8 + kvh) << 22;
  const int rot = team * 16 + (j >> 1) * 4 + (xc >> 1);
  const int koff = (r >> 2) * 512 + g * 64 + (r & 3) * 16, voff = g * 512 + r * 16;
  asm volatile("" :: "s"(x), "s"(K), "s"(V), "s"(wq), "s"(ws), "s"(wo), "s"(wg), "s"(wu), "s"(wd), "s"(out));
  unsigned tag = 0, tagb = 0;
  if (w < 4) {
    SB();
    asm volatile("s_barrier" ::: "memory");
    SB();
  } else {
    const bool act = lane < 56;
    v4u xv, xw; v2u E;
    const unsigned xo = ((w - 4) * DM + (act ? lane : 55) * 8) * 2;
    asm volatile("global_load_dwordx2 %2, %5, %6\n\tglobal_load_dwordx4 %0, %3, %4\n\tglobal_load_dwordx4 %1, %3, %4 offset:3584"
                 : "=&v"(xv), "=&v"(xw), "=&v"(E) : "v"(xo), "s"(x), "v"(WS_E), "s"(ws) : "memory");
    SB();
    const u16* src = wq + (size_t)(qk0 + 8 * (w - 4) + 4 * (lane >> 5) + ((lane >> 1) & 3)) * NQ + xc * 512 + ql * 64 + (lane & 1) * 8 + (((lane >> 3) & 3) ^ (w & 1)) * 16;
#pragma unroll
    for (int I = 0; I < 3; ++I) glds16(src + (size_t)I * 32 * NQ, lbase + L_WQ + (w - 4) * 1024 + I * 4096);
    glds16(src + (size_t)(qd < 2 ? 3 : 2) * 32 * NQ, lbase + L_WQ + (w - 4) * 1024 + 3 * 4096);
    if (lane < 6) LD32(L_FLG + (w - 4) * 24 + lane * 4) = (w - 4) * 6 + lane == 21 ? 4096u : 0u;
    SB();
    asm volatile("s_waitcnt lgkmcnt(0)\n\ts_barrier\n\ts_waitcnt vmcnt(4)" : "+v"(xv), "+v"(xw), "+v"(E) :: "memory");
    SB();
    tag = UNI(E[0]) + 1u; tagb = UNI(E[1]) + 1u;
    float v[8], u[8];
#pragma unroll
    for (int i = 0; i < 4; ++i) { v[2 * i] = lo2f(xv[i]); v[2 * i + 1] = hi2f(xv[i]); u[2 * i] = lo2f(xw[i]); u[2 * i + 1] = hi2f(xw[i]); }
    const v4u xn = ln8(v, act, act), xm = ln8(u, act, act);
    if (act) { LD128(L_XN + (w - 4) * XN_STRIDE + lane * 16) = xn; LD128(L_XN + w * XN_STRIDE + lane * 16) = xm; }
    SB();
    asm volatile("s_waitcnt vmcnt(0)" :: "v"(cpf) : "memory");
    if (lane == 0) VLD32(L_FLG + (w - 4) * 4) = 1u;
  }
#define QGATHER() do { \
    v4u qv[4]; \
    __builtin_amdgcn_s_sleep(GQD); \
    for (int spin = 0; spin < SPIN_CAP; ++spin) { \
      asm volatile("" ::: "memory"); \
      unsigned bad = 0u; \
      _Pragma("unroll") for (int i = 0; i < 4; ++i) qv[i] = ld_dev(rw, qoff + i * 4096); \
      _Pragma("unroll") for (int i = 0; i < 4; ++i) bad |= (qv[i][1] ^ tag) | (qv[i][3] ^ tagb); \
      if (all_ok(bad == 0u)) break; \
    } \
    LD32(qlw) = cvtpk((u2f(qv[0][0]) + u2f(qv[1][0])) + (u2f(qv[2][0]) + u2f(qv[3][0])), (u2f(qv[0][2]) + u2f(qv[1][2])) + (u2f(qv[2][2]) + u2f(qv[3][2]))); \
    asm volatile("" ::: "memory"); \
    if (lane == 0) VLD32(L_FLG + 16 + w * 4) = 1u; } while (0)

#define WSRC() \
    const u16* so = wo + (size_t)(kq * 1024 + 8 * ((lane >> 2) & 3) + 2 * (lane >> 5) + ((lane >> 4) & 1)) * DM + ns * 32 + ((lane & 3) ^ ((lane >> 5) << 1)) * 8; \
    const u16* sg = ((lane & 4) ? wu : wg) + (size_t)(lane >> 3) * FF + fs * 32
#define DMA_GU(I) glds16(sg + (size_t)(I) * 8 * FF + ((lane & 3) ^ ((lane >> 4) & 1) ^ (((I) & 1) << 1)) * 8, lbase + L_GU + (I) * 1024)
#define DMA_WO(I) glds16(so + (((I) >> 1) * 32 + ((I) & 1) * 4) * DM, lbase + L_WO + (I) * 1024)
#define DMAW() asm volatile("s_waitcnt vmcnt(2)" ::: "memory")
  if (w < 4) {
    v4u e[EX][4], bf[4][4];
    const unsigned tb16 = (NOW() + TOFF) * 16u;
    constexpr unsigned h16 = (TEND - TOFF) * 16 / 128;
    unsigned xs = (team * 32 + w * 8 + j) * h16 / 128;
    const int sob = (sp * 256 + w) * 4096;
#define ISSUE(BUF, RS, OFF, N, NW, DUE) do { \
      _Pragma("nounroll") for (int s_ = 0; s_ < 4096 && (int)(NW * 16u - (DUE)) < 0; ++s_) { __builtin_amdgcn_s_sleep(1); NW = NOW(); } \
      const int so_ = sob + (((N) + rot) & 63) * 16384; \
      _Pragma("unroll") for (int i_ = 0; i_ < 4; ++i_) BUF[i_] = __builtin_amdgcn_raw_buffer_load_b128(RS, OFF + (i_ >> 1) * 2048 + (i_ & 1) * 256, so_, 2); \
      xs += h16; \
      SB(); } while (0)
    const __amdgpu_buffer_rsrc_t rk = __builtin_amdgcn_make_buffer_rsrc((void*)(K + base), 0, 4 << 20, 0x00020000);
    {
      unsigned nw = NOW();
      ISSUE(e[0], rk, koff, 0, nw, tb16 + xs);
    }
    mpv = pin(L_MRG + (w & 1) * 1024 + lane * 16); mml = pin(L_OA + (w & 1) * 512 + mh * 8);
    mst = pin(WS_P1 + aidx * 2048 + ((w & 1) * 2 + mh) * 512 + (g * 8 + (lane & 7)) * 16); msl = pin(WS_ML + aidx * 16 + mh * 8);
    const int h = r >> 3, etal = (r >> 2) & 1, sig = r & 3;
    unsigned msk[4];
#pragma unroll
    for (int i = 0; i < 4; ++i) msk[i] = (((lane & 7) >> 1) == i) ? ((lane & 1) ? 0xffff0000u : 0x0000ffffu) : 0u;
    unsigned m1 = (sig & 1) ? ~0u : 0u, m2 = (sig & 2) ? ~0u : 0u;
    asm volatile("" : "+v"(m1), "+v"(m2));
    const int ow = pin(L_MRG + w * 2048 + lane * 16), mw = pin(L_OA + (lane & 1) * 512 + w * 16 + h * 8);
    const int lgw = pin(L_FLG + 64 + w * 4), scw = pin(L_MRG + w * 2048 + (lane >> 2) * 4);
    SB();
#pragma unroll
    for (int s = 1; s < EX + 4; ++s) {
      unsigned nw = NOW();
      if (s < EX) ISSUE(e[s], rk, koff, s, nw, tb16 + xs); else ISSUE(bf[s - EX], rk, koff, s, nw, tb16 + xs);
    }
    LWAIT(L_FLG + 32 + (lane & 3) * 4);
    asm volatile("" :: "v"(cpf));
    SB();
    v4u qb[2][2];
    {
      const v4u q0 = LD128(L_Q + (h * 256 + 32 * g + 8 * sig) * 2), q1 = LD128(L_Q + (h * 256 + 128 + 32 * g + 8 * sig) * 2);
      const v4u z = {0, 0, 0, 0};
      qb[0][0] = etal == 0 ? q0 : z; qb[0][1] = etal == 1 ? q0 : z;
      qb[1][0] = etal == 0 ? q1 : z; qb[1][1] = etal == 1 ? q1 : z;
    }
    f32x4 o0 = {0, 0, 0, 0}, o1 = {0, 0, 0, 0};
    float m = -INFINITY, l = 0.f, gm = -INFINITY;
    constexpr float C2 = 0.0625f * 1.4426950408889634f;
    v2u sch = {0u, 4096u};
    int q;
#define ARRIVED() \
      unsigned due = tb16 + UNI(sch[0]) + ((xs * UNI(sch[1])) >> 12); \
      asm volatile("s_waitcnt vmcnt(%1)" : "+s"(due) : "n"(WV) : "memory"); \
      SB(); \
      unsigned now = NOW(); \
      SB()
#define NEXT(BUF, I) \
      VLD32(lgw) = now * 16u - due; \
      ISSUE(BUF, rn, on, nn + (I), now, due); \
      sch = *(volatile LAS v2u*)(L + L_FLG + 80)
#define KSTEP(KB, SL, PRE, NXT) do { \
      PRE; \
      f32x4 d = {0, 0, 0, 0}; \
      d = mfma(KB[0], qb[0][0], d); d = mfma(KB[1], qb[1][0], d); d = mfma(KB[2], qb[0][1], d); d = mfma(KB[3], qb[1][1], d); \
      asm volatile("" : "+v"(d)); \
      SB(); \
      NXT; \
      SB(); \
      const unsigned sl = (f2u(d[1]) & m1) | (f2u(d[0]) & ~m1), sh = (f2u(d[3]) & m1) | (f2u(d[2]) & ~m1); \
      float sc = u2f((sh & m2) | (sl & ~m2)); \
      sc += dpp<XOR1>(sc); sc += dpp<XOR2>(sc); \
      sc = rbf(sc); \
      gm = fmaxf(gm, sc); \
      VLD32(SL) = f2u(sc); \
      SB(); } while (0)
#define VSTEP(VB, SL, PRE, NXT) do { \
      const float p = __builtin_amdgcn_exp2f(fmaf(u2f(VLD32(SL)), C2, -m)); \
      l += p; \
      const unsigned pw = cvtpk(p, p), pwo = dppu<HMIR>(pw); \
      v4u b0 = {pw & msk[0], pw & msk[1], pwo & msk[2], pwo & msk[3]}; \
      v4u b1 = {pwo & msk[0], pwo & msk[1], pw & msk[2], pw & msk[3]}; \
      asm volatile("" : "+v"(b0), "+v"(b1)); \
      PRE; \
      o0 = mfma(VB[0], b0, o0); o1 = mfma(VB[1], b0, o1); o0 = mfma(VB[2], b1, o0); o1 = mfma(VB[3], b1, o1); \
      asm volatile("" : "+v"(o0), "+v"(o1)); \
      SB(); \
      NXT; \
      SB(); } while (0)
#define RUN(q) \
      const bool tv = ((q) + 4) & GS; \
      const __amdgpu_buffer_rsrc_t rn = __builtin_amdgcn_make_buffer_rsrc((void*)((tv ? V : K) + base), 0, 4 << 20, 0x00020000); \
      const int on = tv ? voff : koff, nn = (((q) + 4) >> 1 & ~(GS - 1)) | (((q) + 4) & (GS - 1))
    {
      RUN(EX);
#pragma unroll
      for (int i = 0; i < 4; ++i) {
        KSTEP(bf[i], scw + (EX + i) * 64, ARRIVED(), NEXT(bf[i], i));
#pragma unroll
        for (int u = 0; u < EX / 4; ++u) KSTEP(e[i * (EX / 4) + u], scw + (i * (EX / 4) + u) * 64, , );
      }
    }
    q = EX + 4;
#pragma nounroll
    while (true) {
#pragma nounroll
      do {
        RUN(q);
        const int sa_ = scw + (q & (GS - 1)) * 64;
#pragma unroll
        for (int i = 0; i < 4; ++i) KSTEP(bf[i], sa_ + i * 64, ARRIVED(), NEXT(bf[i], i));
        q += 4;
      } while (q & (GS - 1));
      {
        float pm = fmaxf(gm, dpp<HMIR>(gm)); pm = xrow_max(pm);
        const float mn = fmaxf(m, pm * C2);
        const float alpha = __builtin_amdgcn_exp2f(m - mn);
        m = mn; l *= alpha; o0 *= alpha; o1 *= alpha;
      }
#pragma nounroll
      do {
        RUN(q);
        const int sa_ = scw + (q & (GS - 1)) * 64;
#pragma unroll
        for (int i = 0; i < 4; ++i) VSTEP(bf[i], sa_ + i * 64, ARRIVED(), NEXT(bf[i], i));
        q += 4;
      } while ((q & (GS - 1)) && q != 124);
      if (q == 124) break;
    }
#pragma unroll
    for (int i = 0; i < 4; ++i)
      VSTEP(bf[i], scw + (GS - 4 + i) * 64, if (i == 3) {
        SB();
        l = l + dpp<HMIR>(l); l = xrow_sum(l);
        if (g == 0 && (lane & 6) == 0) LD64(mw) = (v2u{f2u(m), f2u(l)});
        SB();
      }, );
    LD128(ow) = __builtin_bit_cast(v4u, o0);
    LD128(ow + 1024) = __builtin_bit_cast(v4u, o1);
  } else if (w == 4) {
    const int a0 = pin(L_WQ + g * 1024 + (g & 1) * 128 + r * 8), a1 = pin(L_WQ + g * 1024 + (1 - (g & 1)) * 128 + r * 8);
    const int bo = pin(L_XN + (r & 7) * XN_STRIDE + qk0 * 2 + g * 16);
    const unsigned qst = pin(WS_QG + ((((r & 7) * 8 + xc) * 4 + qd) * 256 + ql * 32 + 2 * g + (r >> 3)) * 16);
    const unsigned m8 = pin((r & 8) ? ~0u : 0u);
    const unsigned qoff = pin(WS_QG + ((b * 8 + kvh) * 1024 + lane) * 16);
    const int qlw = pin(L_Q + lane * 4);
    LWAIT(L_FLG + (lane & 3) * 4);
    v4u wa[4][4];
#pragma unroll
    for (int ks = 0; ks < 4; ++ks)
#pragma unroll
      for (int t = 0; t < 4; ++t) {
        const int a = ((t & 1) ? a1 : a0) + ks * 4096 + (t >> 1) * 256;
        const v2u t0 = TR(a), t1 = TR(a + 512); wa[ks][t] = v4u{t0[0], t0[1], t1[0], t1[1]};
      }
    f32x4 d[4] = {{0, 0, 0, 0}, {0, 0, 0, 0}, {0, 0, 0, 0}, {0, 0, 0, 0}};
    v4u bb[4];
#pragma unroll
    for (int ks = 0; ks < 4; ++ks) bb[ks] = LD128(bo + ks * 64);
    SB();
#pragma unroll
    for (int ks = 0; ks < 3; ++ks)
#pragma unroll
      for (int t = 0; t < 4; ++t) d[t] = mfma(wa[ks][t], bb[ks], d[t]);
    if (qd < 2) {
#pragma unroll
      for (int t = 0; t < 4; ++t) d[t] = mfma(wa[3][t], bb[3], d[t]);
    }
#pragma unroll
    for (int t = 0; t < 4; ++t)
      st_dev(rw, qst + t * 128, v4u{(f2u(d[t][2]) & m8) | (f2u(d[t][0]) & ~m8), tag, (f2u(d[t][3]) & m8) | (f2u(d[t][1]) & ~m8), tagb});
    asm volatile("" ::: "memory");
    if (lane == 0) VLD32(L_FLG + 16) = 1u;
    QGATHER();
    float sa = 0.f, ss = 1.f;
    unsigned last = 0;
#pragma nounroll
    for (int k = 0; k < NK; ++k) {
      const unsigned tc = tk0 + TOFF + C0 + k * CS;
      int own = 0;
#pragma nounroll
      for (int i = 0; i < 3; ++i) {
        TWAIT(tc - 2 * SW + i * SW, 8);
        const v4u lw = *(volatile LAS v4u*)(L + L_FLG + 64);
        own += (int)lw[0] + (int)lw[1] + (int)lw[2] + (int)lw[3];
      }
      if (((j + xc + team) & 3) == 0 && lane < 4)
        st_dev(rw, WS_PB + k * 4096 + (team * 16 + (j >> 2) * 8 + xc) * 64 + lane * 16, v4u{tag, (unsigned)own, tag, (unsigned)own});
#pragma nounroll
      for (int i = 0; i < 3; ++i) {
        TWAIT(tc + PT + i * 100 + (bid >> 3), 8);
        const v2u f = ld_dev8(rw, WS_RS + xc * 128);
        const unsigned f1 = UNI(f[1]);
        if (UNI(f[0]) != tag) continue;
        if ((f1 ^ last) & 0xffffffu) {
          const unsigned dl = f1 & 0xfffu, jd = ((f1 >> 12) & 0xfffu) - ((last >> 12) & 0xfffu);
          const float p = (float)(int)((NOW() - tk0 - 75 - TOFF) * 16u), xe = (TEND - TOFF) * 16.f;
          const float xp = (p - sa - (float)(int)(jd * 16u)) * __builtin_amdgcn_rcpf(ss);
          ss = (xe + (float)(dl * 16u) - p) * __builtin_amdgcn_rcpf(xe - xp); sa = p - xp * ss; last = f1;
          if (lane == 0) { *(volatile LAS v2u*)(L + L_FLG + 80) = v2u{(unsigned)(int)sa, (unsigned)(ss * 4096.f)}; VLD32(L_FLG + 88) = dl; }
        }
        if ((int)(f1 >> 24) == k) break;
      }
      if (k == 0) {
        const int nchk = (bid == 56) ? 4 : 1;
        for (int i = 0; i < nchk; ++i) {
          const unsigned off = WS_CF + (((team + i) & 3) * 64 + lane) * CFS;
#pragma nounroll
          for (int spin = 0; spin < SPIN_CAP; ++spin) {
            asm volatile("" ::: "memory");
            const v2u f = ld_dev8(rw, off);
            if (all_ok(f[0] == tag && f[1] == tagb)) break;
            __builtin_amdgcn_s_sleep(32);
          }
        }
        if (bid == 56 && lane == 0) st_dev8(rw, WS_E, v2u{tag, tagb});
      }
    }
  } else {
    const unsigned td = NOW() + TD;
    const unsigned qoff = pin(WS_QG + ((b * 8 + kvh) * 1024 + (w - 4) * 64 + lane) * 16);
    const int qlw = pin(L_Q + ((w - 4) * 64 + lane) * 4);
    if (w == 7) {
      const v4u c = {SENT, SENT, SENT, SENT};
#pragma unroll
      for (int i = 0; i < 2; ++i) st_dev(rw, WS_P1 + aidx * 2048 + i * 1024 + lane * 16, c);
      if (lane == 0) st_dev(rw, WS_ML + aidx * 16, c);
      if (j < 7) {
#pragma unroll
        for (int i = 0; i < NC / 4; ++i)
          st_dev(rw, WS_P2 + (4 * i + g) * P2C + (((team * 2 + ((lane >> 3) & 1)) * 4 + kq) * DM + ns * 32 + (lane & 7) * 4) * 4, c);
#pragma unroll
        for (int i = 0; i < 4; ++i)
          if (i < 3 || lane < 32) st_dev(rw, WS_P3 + (team * 56 + fs) * 3584 + i * 1024 + lane * 16, c);
      }
    }
    LWAIT(L_FLG + 16);
    QGATHER();
    if (w == 7) {
      asm volatile("s_waitcnt vmcnt(0)" ::: "memory");
      if (lane == 0) st_dev8(rw, WS_CF + bid * CFS, v2u{tag, tagb});
    }
    if (bid == 57 && w == 5) {
      float dl = 0.f, ja = 0.f, xp = 0.f;
#pragma nounroll
      for (int k = 0; k < NK; ++k) {
        float sm, ct;
#pragma nounroll
        for (int i = 0; i < 4; ++i) {
          TWAIT(tk0 + TOFF + C0 + k * CS + 150 + i * 100, 8);
          const v2u f = ld_dev8(rw, WS_PB + k * 4096 + lane * 64);
          sm = wave_sum(f[0] == tag ? (float)(int)f[1] : 0.f); ct = wave_sum(f[0] == tag ? 1.f : 0.f);
          if (UNI(f2u(ct)) >= 0x42400000u) break;
        }
        const float ml = sm * __builtin_amdgcn_rcpf(ct * (12.f * 16.f)), x = ml - FL;
        if (ct >= 32.f) {
          const bool big = x > XS;
          const float sl = (!big || xp > XS) ? GR * (fminf(x, XC) - X0) * (float)(TEND - C0 - k * CS) * (1.f / 5600.f) : 0.f;
          const float jp = fminf(big ? x : fminf(JC * x, XS), DMX - dl);
          if (big || (x > X0 && x > HD * xp)) { ja += jp; dl = fminf(dl + jp + sl, DMX); }
          if (lane < 8) st_dev8(rw, WS_RS + lane * 128, v2u{tag, (unsigned)dl | ((unsigned)ja << 12) | ((unsigned)k << 24)});
        }
        xp = ct >= 32.f ? x : 0.f;
      }
    }
    if (j < 7) {
      WSRC();
      for (int I = 7 - w; I < 120; I += 3) {
        TWAIT(td + I * DT, 4);
        if (I < 64) DMA_WO(I); else DMA_GU(I - 64);
        DMAW();
      }
      for (int I = 7 - w; I < 28; I += 3) { TWAIT(td + (120 + I) * DT, 4); glds16(wd + (size_t)fs * 32 * DM + I * 512 + lane * 8, lbase + L_WD + I * 1024); DMAW(); }
      if (w < 7) {
        const u16* xr = x + (2 * team + (w - 5)) * DM + lane * 4;
        const v2u xa = *(const v2u*)xr, xb = *(const v2u*)(xr + (lane < 48 ? 256 : 0));
        xres = v4u{xa[0], xa[1], xb[0], xb[1]};
      }
    }
    __builtin_amdgcn_s_waitcnt(0x0f70);
    asm volatile("" ::: "memory");
  }
  __builtin_amdgcn_s_waitcnt(0x0f70);
  BAR();
  const unsigned tdir = tk0 + TEND + H1T + UNI(LD32(L_FLG + 88));
  if (w < 2) {
    v4u pv[4]; v2u ml[4];
#pragma unroll
    for (int i = 0; i < 4; ++i) ml[i] = LD64(mml + i * 16);
#pragma unroll
    for (int i = 0; i < 4; ++i) pv[i] = LD128(mpv + i * 2048);
    SB();
    const float M = fmaxf(fmaxf(u2f(ml[0][0]), u2f(ml[1][0])), fmaxf(u2f(ml[2][0]), u2f(ml[3][0])));
    f32x4 a = {0, 0, 0, 0}; float Ls = 0.f;
#pragma unroll
    for (int i = 0; i < 4; ++i) {
      const float f = __builtin_amdgcn_exp2f(u2f(ml[i][0]) - M);
      a += f * __builtin_bit_cast(f32x4, pv[i]); Ls = fmaf(f, u2f(ml[i][1]), Ls);
    }
    st_dev(rw, mst, words(a));
    if (w == 0 && (lane & 0x37) == 0) st_dev8(rw, msl, v2u{word(M), word(Ls)});
  }
  if (j == 7) return;
  const int psb = pin(L_OA + lane * 4);
  const unsigned pst = pin(WS_P2 + (((team * 2 + (lane >> 5)) * 4 + kq) * DM + ns * 32 + (lane & 31)) * 4);
  const int hnb = pin(L_HN + (r & 1) * XN_STRIDE + g * 16), hhw = pin(L_HH + (r & 1) * 64 + (w & 3) * 16 + (g & 1) * 8);
  const int hhr = pin(L_HH + ((r >> 2) & 1) * 64 + g * 16);
  const unsigned p3s = pin(WS_P3 + ((team * 56 + ((r >> 2) & 1) * 28 + 4 * w + g) * 56 + fs) * 64 + (r & 3) * 16);
  {
    v4u wa[4][2];
#pragma unroll
    for (int s = 0; s < 4; ++s)
#pragma unroll
      for (int c = 0; c < 2; ++c) {
        const int a = L_WO + (4 * w + s) * 2048 + g * 512 + (r >> 2) * 64 + (c ^ (g & 1)) * 32 + (r & 3) * 8;
        const v2u t0 = TR(a), t1 = TR(a + 256);
        wa[s][c] = v4u{t0[0], t0[1], t1[0], t1[1]};
      }
    const int a0 = ((2 * team) * 8 + 2 * kq + (w >> 2)) * 4, hh = (w >> 1) & 1, up = lane >> 5;
    const unsigned doff = WS_P1 + (a0 + up) * 2048 + ((w & 1) * 2 + hh) * 512 + (lane & 31) * 16;
    const unsigned moff = WS_ML + (a0 + ((lane >> 2) & 1) * 32 + ((lane & 3) ^ up)) * 16 + hh * 8;
    const int oaw = pin(L_OA + w * 512 + lane * 8), oar = pin(L_OA + w * 512 + (r & 1) * 256 + g * 16);
    const int oas = pin(L_OA + w * 512 + (r & 1) * 128 + ((r >> 1) & 1) * 64 + g * 16);
    v4u v[4]; v2u ml = {SENT, SENT};
    unsigned pend = 0xfu, seen = 0u, want = (int)(NOW() - tdir) >= 0 ? 0xfu : 0u;
#pragma unroll
    for (int i = 0; i < 4; ++i) v[i] = v4u{SENT, SENT, SENT, SENT};
    for (int spin = 0; spin < SPIN_CAP; ++spin) {
      asm volatile("" ::: "memory");
      const unsigned tn = NOW();
      if (seen != 0xffu) ml = ld_dev8(rw, moff);
#pragma unroll
      for (int i = 0; i < 4; ++i)
        if ((want >> i) & 1u) v[i] = ld_dev(rw, doff + (i >> 1) * (32 * 2048) + (i & 1) * 4096);
      const unsigned tm = umax(ml[0], ml[1]);
      unsigned t[4];
#pragma unroll
      for (int i = 0; i < 4; ++i) t[i] = top(v[i]);
      if (all_ok(umax(umax(tm, t[0]), umax(umax(t[1], t[2]), t[3])) != SENT)) break;
      seen = (unsigned)__builtin_amdgcn_ballot_w64(tm != SENT) & 0xffu;
#pragma unroll
      for (int i = 0; i < 4; ++i)
        if (all_ok(t[i] != SENT)) pend &= ~(1u << i);
      const unsigned s2 = seen & (seen >> 1);
      const unsigned both = (s2 & 1u) | ((s2 >> 1) & 2u) | ((s2 >> 2) & 4u) | ((s2 >> 3) & 8u);
      want = (__builtin_popcount(pend) <= 1) ? pend : (pend & both);
      if ((int)(tn - tdir) >= 0) want = pend;
      if (want == 0u) __builtin_amdgcn_s_sleep(1);
    }
    float M = fmaxf(u2f(ml[0]), dpp<XOR1>(u2f(ml[0]))); M = fmaxf(M, dpp<XOR2>(M));
    const float f = __builtin_amdgcn_exp2f(u2f(ml[0]) - M);
    float Ls = f * u2f(ml[1]); Ls += dpp<XOR1>(Ls); Ls += dpp<XOR2>(Ls);
    const float cf = f * __builtin_amdgcn_rcpf(Ls);
    const float c0 = dpp<BCAST + 0>(cf), c1 = dpp<BCAST + 2>(cf), c2 = dpp<BCAST + 4>(cf), c3 = dpp<BCAST + 6>(cf);
    float y[4];
#pragma unroll
    for (int i = 0; i < 4; ++i) {
      const float xa = fmaf(c1, u2f(v[1][i]), c0 * u2f(v[0][i])), xb = fmaf(c3, u2f(v[3][i]), c2 * u2f(v[2][i]));
      auto s = __builtin_amdgcn_permlane32_swap(f2u(xa), f2u(xb), false, false);
      y[i] = u2f(s[0]) + u2f(s[1]);
    }
    LD64(oaw) = v2u{cvtpk(y[0], y[1]), cvtpk(y[2], y[3])};
    f32x4 d0 = {0, 0, 0, 0}, d1 = {0, 0, 0, 0};
    v4u bb[4];
    blank(bb);
    if (r < 4) {
#pragma unroll
      for (int s = 0; s < 4; ++s) bb[s] = LD128(oar + s * 64);
    }
    SB();
#pragma unroll
    for (int s = 0; s < 4; ++s) { d0 = mfma(wa[s][0], bb[s], d0); d1 = mfma(wa[s][1], bb[s], d1); }
    const f32x4 ds = (r & 2) ? d1 : d0;
    if (r < 4) LD128(oas) = __builtin_bit_cast(v4u, ds);
  }
  BAR();
  v4u ga[14];
  v4u da[4];
  {
    float pa[8];
#pragma unroll
    for (int i = 0; i < 8; ++i) pa[i] = u2f(LD32(psb + i * 512));
    const float a = ((pa[0] + pa[1]) + (pa[2] + pa[3])) + ((pa[4] + pa[5]) + (pa[6] + pa[7]));
    if (w < NC) __builtin_amdgcn_raw_buffer_store_b32(word(a), rw, pst, w * P2C, 16);
    SB();
  }
  if (w < 7) {
#pragma unroll
    for (int t = 0; t < 4; ++t) {
      const int a = L_WD + (8 * g + (r >> 2)) * (DM * 2) + w * 128 + (r & 3) * 32 + t * 8;
      const v2u t0 = TR(a), t1 = TR(a + 4 * DM * 2);
      da[t] = v4u{t0[0], t0[1], t1[0], t1[1]};
    }
  }
  SB();
  if (w == 5 || w == 6) {
    v4u v[8];
    const unsigned poff = WS_P2 + (xc & (NC - 1)) * P2C + (team * 2 + (w - 5)) * (4 * DM * 4) + lane * 16, p1 = lane < 48 ? 1024u : OOR;
    unsigned po[8];
#pragma unroll
    for (int i = 0; i < 8; ++i) po[i] = poff + (i >> 1) * (DM * 4) + ((i & 1) ? p1 : 0u);
    const int hw = pin(L_HN + (w - 5) * XN_STRIDE + lane * 8), hr = pin(L_HRES + (w - 5) * (DM * 2) + lane * 8);
    float xf[8];
#pragma unroll
    for (int i = 0; i < 4; ++i) { xf[2 * i] = pin(lo2f(xres[i])); xf[2 * i + 1] = pin(hi2f(xres[i])); }
    SB(); __builtin_amdgcn_s_sleep(4); SB();
    gather<8>(rw, po, v);
    float hv[8];
#pragma unroll
    for (int i = 0; i < 4; ++i) {
      const int u = i >> 1, e = (i & 1) * 2;
      const float s0 = (u2f(v[u][e]) + u2f(v[2 + u][e])) + (u2f(v[4 + u][e]) + u2f(v[6 + u][e]));
      const float s1 = (u2f(v[u][e + 1]) + u2f(v[2 + u][e + 1])) + (u2f(v[4 + u][e + 1]) + u2f(v[6 + u][e + 1]));
      const unsigned at = cvtpk(s0, s1);
      xres[i] = cvtpk(xf[2 * i] + lo2f(at), xf[2 * i + 1] + hi2f(at));
      hv[2 * i] = lo2f(xres[i]); hv[2 * i + 1] = hi2f(xres[i]);
    }
    const v4u hn = ln8(hv, true, lane < 48);
    LD64(hw) = v2u{hn[0], hn[1]};
    LD64(hr) = v2u{xres[0], xres[1]};
    if (lane < 48) {
      LD64(hw + 512) = v2u{hn[2], hn[3]};
      LD64(hr + 512) = v2u{xres[2], xres[3]};
    }
  } else if (w < 4) {
    const int ab = pin(L_GU + g * 1024 + (r >> 2) * 128 + ((r >> 1) & 1) * 64 + (w ^ (r >> 3) ^ ((g & 1) << 1)) * 16 + (r & 1) * 8);
#pragma unroll
    for (int ks = 0; ks < 14; ++ks) {
      const int a = ab + ks * 4096;
      const v2u t0 = TR(a), t1 = TR(a + 512);
      ga[ks] = v4u{t0[0], t0[1], t1[0], t1[1]};
    }
  }
  BAR();
  if (w < 4) {
    f32x4 d0 = {0, 0, 0, 0}, d1 = {0, 0, 0, 0};
    v4u bb[14];
    blank(bb);
    if (r < 2) {
#pragma unroll
      for (int ks = 0; ks < 14; ++ks) bb[ks] = LD128(hnb + ks * 64);
    }
    SB();
#pragma unroll
    for (int ks = 0; ks < 14; ++ks) { if (ks & 1) d1 = mfma(ga[ks], bb[ks], d1); else d0 = mfma(ga[ks], bb[ks], d0); }
    d0 += d1;
    const unsigned p0 = cvtpk(d0[0], d0[1]), p1 = cvtpk(d0[2], d0[3]);
    auto s0 = __builtin_amdgcn_permlane32_swap(p0, p0, false, false);
    auto s1 = __builtin_amdgcn_permlane32_swap(p1, p1, false, false);
    const unsigned h0 = cvtpk(lo2f(s0[0]) * lo2f(s0[1]), hi2f(s0[0]) * hi2f(s0[1]));
    const unsigned h1 = cvtpk(lo2f(s1[0]) * lo2f(s1[1]), hi2f(s1[0]) * hi2f(s1[1]));
    if (g < 2 && r < 2) LD64(hhw) = v2u{h0, h1};
  }
  BAR();
  if (w < 7) {
    const v4u bb = LD128(hhr);
    const f32x4 z = {0, 0, 0, 0};
    const f32x4 e0 = mfma(da[0], bb, z), e1 = mfma(da[1], bb, z), e2 = mfma(da[2], bb, z), e3 = mfma(da[3], bb, z);
    const f32x4 e = (r & 2) ? ((r & 1) ? e3 : e2) : ((r & 1) ? e1 : e0);
    if (r < 8) st_dev(rw, p3s, words(e));
  } else {
    const int oe = 4 * (lane & 3) + ((lane >> 3) & 2) + (lane >> 5);
    const float hr = pin(lo2f(*(LAS u16*)(L + L_HRES + fs * 32 + oe * 2)));
    const unsigned op = pin((unsigned)(team * 896 + fs * 16 + oe) * 2u);
    v4u v[4];
    const unsigned foff = WS_P3 + (team * 56 + fs) * 3584 + lane * 16, f3 = lane < 32 ? 3072u : OOR;
    const unsigned fo[4] = {foff, foff + 1024u, foff + 2048u, foff + f3};
    SB(); __builtin_amdgcn_s_sleep(20); SB();
    gather<4>(rw, fo, v);
    float t[4];
#pragma unroll
    for (int i = 0; i < 4; ++i) t[i] = (u2f(v[0][i]) + u2f(v[1][i])) + (u2f(v[2][i]) + u2f(v[3][i]));
    const auto sa = __builtin_amdgcn_permlane32_swap(f2u(t[0]), f2u(t[1]), false, false);
    const auto sb = __builtin_amdgcn_permlane32_swap(f2u(t[2]), f2u(t[3]), false, false);
    const auto sc = __builtin_amdgcn_permlane16_swap(f2u(u2f(sa[0]) + u2f(sa[1])), f2u(u2f(sb[0]) + u2f(sb[1])), false, false);
    float y = u2f(sc[0]) + u2f(sc[1]);
    y += dpp<ROR8>(y); y += dpp<ROR4>(y);
    const float o = hr + rbf(y);
    if ((lane & 12) == 0) *(u16*)((char*)out + op) = (u16)cvtpk(o, o);
    asm volatile("Lcode1:");
  }
}

extern "C" void kernel_launch(void* const* d_in, const int* in_sizes, int n_in, void* d_out, int out_size, void* d_ws, size_t ws_size,
                              hipStream_t stream) {
  fused<<<256, 512, 0, stream>>>((const u16*)d_in[0], (const char*)d_in[1], (const char*)d_in[2], (const u16*)d_in[3], (char*)d_ws,
                                 (const u16*)d_in[4], (const u16*)d_in[6], (const u16*)d_in[7], (const u16*)d_in[8], (u16*)d_out);
}
```
